```python
import torch

def _softmax(x, axis=-1):
    return torch.softmax(x, dim=axis)
def _gelu(x):
    return torch.nn.functional.gelu(x, approximate="tanh")
def _silu(x):
    return torch.nn.functional.silu(x)
def _sigmoid(x):
    return torch.sigmoid(x)
def _layernorm(x, axis=-1, eps=1e-5):
    m = x.mean(dim=axis, keepdim=True)
    v = x.var(dim=axis, keepdim=True, unbiased=False)
    return (x - m) / torch.sqrt(v + eps)
def _scatter_add(tgt, idx, src, axis=0):
    return tgt.clone().index_add_(axis, idx, src)
def _segment_sum(src, seg, n):
    out = torch.zeros((n,) + src.shape[1:], dtype=src.dtype)
    return out.index_add_(0, seg, src)
def _pad(x, pw):
    flat = tuple(v for pair in reversed(pw) for v in pair)
    return torch.nn.functional.pad(x, flat)

kernel_name = 'standalone__gqa_block_cached__sampling'

def setup_inputs():

    _w = (lambda a: (lambda t: t.to(torch.bfloat16) if t.is_floating_point() else t)(torch.as_tensor(a)))
    B, Sq, D = 32, 16, 1024
    Hq, Hkv, Sk, Dh = 32, 4, 2048, 128
    assert Hq % Hkv == 0, (Hq, Hkv)
    return {
        "x": _w((lambda *s: torch.randn(*s, dtype=torch.bfloat16) * 0.8)(B, Sq, D)),
        "k_cache": _w((lambda *s: torch.randn(*s, dtype=torch.bfloat16) * 0.8)(B, Hkv, Sk, Dh)),
        "v_cache": _w((lambda *s: torch.randn(*s, dtype=torch.bfloat16) * 0.05)(B, Hkv, Sk, Dh)),
        "wq": _w((lambda *s: torch.randn(*s, dtype=torch.bfloat16) * 0.8)(D, Hq * Dh) * (D ** -0.5)),
        "wo": _w((lambda *s: torch.randn(*s, dtype=torch.bfloat16) * 0.05)(Hq * Dh, D) * ((Hq * Dh) ** -0.5)),
    }

def reference(x, k_cache, v_cache, wq, wo):
    B, Sq, D = x.shape
    Hq, Dh = 32, 128
    Hkv = k_cache.shape[1]
    rep = Hq // Hkv
    q = (lambda x, *d: x.permute(*d))((x @ wq).reshape(B, Sq, Hq, Dh), 0, 2, 1, 3)
    k = (lambda x, n, axis: torch.repeat_interleave(x, n, dim=axis))(k_cache, rep, axis=1)
    v = (lambda x, n, axis: torch.repeat_interleave(x, n, dim=axis))(v_cache, rep, axis=1)
    scores = torch.einsum("bhqd,bhkd->bhqk", q, k) * (Dh ** -0.5)
    p = _softmax(scores, axis=-1)
    o = torch.einsum("bhqk,bhkd->bhqd", p, v)
    return (lambda x, *d: x.permute(*d))(o, 0, 2, 1, 3).reshape(B, Sq, Hq * Dh) @ wo
```

```cpp
#define ATTN_B 32
#define ATTN_HQ 32
#define ATTN_HKV 4
#define ATTN_SQ 16
#define ATTN_SK 2048
#define ATTN_NSPLIT 1
#include <hip/hip_runtime.h>
#include <hip/hip_bf16.h>
#include <cstdio>

#define ATTN_THR 8
#define ATTN_KVCONV 0
#define ATTN_CAUSAL 0
#define ATTN_LENS 0
#define ATTN_QPARK (4 + ATTN_LENS)
#define ATTN_WG4 1
#define ATTN_QPARK4 4
#define ATTN_XCHG4 1
constexpr int B = ATTN_B, HQ = ATTN_HQ, HKV = ATTN_HKV, SQ = ATTN_SQ, SK = ATTN_SK;
constexpr int NSPLIT = ATTN_NSPLIT;
constexpr float THR = ATTN_THR;
constexpr int CAUSAL = ATTN_CAUSAL;
constexpr int HAS_LENS = ATTN_LENS;
constexpr int KV_CONVERT = ATTN_KVCONV;
constexpr int QPARK = ATTN_QPARK;
constexpr int WG4 = ATTN_WG4;
constexpr int XCHG4 = ATTN_XCHG4;
constexpr int QPARK4 = ATTN_QPARK4;
constexpr int D = 128, NW = 8, QBLK = 32, KVBLK = 64;
constexpr float SCALE = 0.088388347648318447f;
constexpr int G = HQ / HKV, ROWS = G * SQ, NT = SK / KVBLK, TPS = NT / NSPLIT;
constexpr int NWB = (WG4 && ROWS <= QBLK * 4 && !CAUSAL && !HAS_LENS && TPS % 4 == 0) ? 4 : NW, NSB = NSPLIT * (NW / NWB), TPSB = NT / NSB;
static_assert(HQ % HKV == 0 && ROWS >= 1 && ROWS <= QBLK * NW, "G*SQ must fit the workgroup's 256 MFMA rows (larger SQ -> dense body)");
static_assert(!(CAUSAL && HAS_LENS && !KV_CONVERT), "CAUSAL + HAS_LENS on the f32 convert-on-load loop spills 2 VGPRs (ROCm 7.2, QPARK 4..8): set KV_CONVERT 1 (bf16 loop, 0 spill) for that combination");
static_assert(SK % KVBLK == 0 && NT % NSPLIT == 0 && TPS >= 2 && TPS % 2 == 0, "SK must give every split an even number (>= 2) of 64-key tiles");
static_assert(NT % NSB == 0 && TPSB >= 2 && TPSB % 2 == 0, "internal: half-split tile count (NWB falls back to 8 waves when TPS % 4 != 0)");
constexpr float L2E = 1.4426950408889634f, SL2E = SCALE * L2E, THR2 = THR * L2E;
constexpr size_t SHM_V = KVBLK * D * 2, SHM_K = KVBLK * D * 2, SHM_WS = 2 * SHM_V + 2 * SHM_K;
template <int NWT> constexpr size_t shm_qp() { return SHM_WS + ((NWT == NW || !XCHG4) ? (size_t)NWT * 64 * 4 : 0); }
static_assert(QPARK >= 0 && QPARK <= 8 && QPARK4 >= 0 && QPARK4 <= 8, "QPARK");
constexpr size_t WS_PML = 4096, PML_BYTES = (size_t)B * HQ * SQ * NSB * 2 * 4;
constexpr size_t WS_PO = (WS_PML + PML_BYTES + 255) / 256 * 256, WS_PEND = NSB > 1 ? WS_PO + (size_t)B * HQ * SQ * NSB * D * 4 : WS_PML;
constexpr size_t NQ = (size_t)B * HQ * SQ * D, NKV = (size_t)B * HKV * SK * D;
constexpr size_t WS_QB = (WS_PEND + 255) / 256 * 256, WS_KB = WS_QB + NQ * 2, WS_VB = WS_KB + NKV * 2;
constexpr size_t WS_END = KV_CONVERT ? WS_VB + NKV * 2 : WS_PEND;

using bf16 = __hip_bfloat16;
using bf16x8 = __attribute__((ext_vector_type(8))) short;
using s16x4 = __attribute__((ext_vector_type(4))) short;
using f32x16 = __attribute__((ext_vector_type(16))) float;
using f32x4 = __attribute__((ext_vector_type(4))) float;
using u32x4 = __attribute__((ext_vector_type(4))) unsigned;
template <class T> struct InCfg;
template <> struct InCfg<__hip_bfloat16> { static constexpr int NWT = NWB, QP = NWB == 4 ? QPARK4 : 0; };
template <> struct InCfg<float> { static constexpr int NWT = NW, QP = QPARK; };
template <class T> constexpr int nw_for() { return InCfg<T>::NWT; }
template <class T> constexpr int ns_for() { return NSPLIT * (NW / InCfg<T>::NWT); }
template <class T> constexpr size_t shm_bytes() { return shm_qp<InCfg<T>::NWT>() + (size_t)InCfg<T>::QP * 64 * InCfg<T>::NWT * 16; }
#define KSWZ(row, colB) ((row) * 256 + ((colB) ^ (((row) & 7) << 4)))
#define SBAR() __builtin_amdgcn_sched_barrier(0)
__device__ __forceinline__ int crow(int r, int hi) { return (r & 3) + 8 * (r >> 2) + 4 * hi; }
__device__ __forceinline__ unsigned cvtpk(float lo, float hi) { unsigned r; asm volatile("v_cvt_pk_bf16_f32 %0, %1, %2" : "=v"(r) : "v"(lo), "v"(hi)); return r; }
__device__ __forceinline__ bf16x8 pack8(f32x4 a, f32x4 b) { u32x4 w = {cvtpk(a[0], a[1]), cvtpk(a[2], a[3]), cvtpk(b[0], b[1]), cvtpk(b[2], b[3])}; return *reinterpret_cast<bf16x8*>(&w); }
__device__ __forceinline__ bf16x8 ld8(const bf16* p) { return *reinterpret_cast<const bf16x8*>(p); }
__device__ __forceinline__ bf16x8 ld8(const float* p) { return pack8(*reinterpret_cast<const f32x4*>(p), *reinterpret_cast<const f32x4*>(p + 4)); }
template <class T, int NR> struct Stage;
template <> struct Stage<bf16, 2> { bf16x8 v0, v1, k0, k1;
  __device__ __forceinline__ void load(const bf16* V0, const bf16* V1, const bf16* K0, const bf16* K1) { v0 = ld8(V0); v1 = ld8(V1); k0 = ld8(K0); k1 = ld8(K1); }
  __device__ __forceinline__ bf16x8 gv0() const { return v0; } __device__ __forceinline__ bf16x8 gv1() const { return v1; }
  __device__ __forceinline__ bf16x8 gk0() const { return k0; } __device__ __forceinline__ bf16x8 gk1() const { return k1; } };
template <> struct Stage<float, 2> { f32x4 v0a, v0b, v1a, v1b, k0a, k0b, k1a, k1b;
  __device__ __forceinline__ void load(const float* V0, const float* V1, const float* K0, const float* K1) {
    v0a = *(const f32x4*)V0; v0b = *(const f32x4*)(V0 + 4); v1a = *(const f32x4*)V1; v1b = *(const f32x4*)(V1 + 4);
    k0a = *(const f32x4*)K0; k0b = *(const f32x4*)(K0 + 4); k1a = *(const f32x4*)K1; k1b = *(const f32x4*)(K1 + 4); }
  __device__ __forceinline__ bf16x8 gv0() const { return pack8(v0a, v0b); } __device__ __forceinline__ bf16x8 gv1() const { return pack8(v1a, v1b); }
  __device__ __forceinline__ bf16x8 gk0() const { return pack8(k0a, k0b); } __device__ __forceinline__ bf16x8 gk1() const { return pack8(k1a, k1b); } };
template <int NR> struct Stage<bf16, NR> { bf16x8 v[NR], k[NR];
  __device__ __forceinline__ void ldv(int i, const bf16* p) { v[i] = ld8(p); } __device__ __forceinline__ void ldk(int i, const bf16* p) { k[i] = ld8(p); }
  __device__ __forceinline__ bf16x8 gv(int i) const { return v[i]; } __device__ __forceinline__ bf16x8 gk(int i) const { return k[i]; } };
template <int NR> struct Stage<float, NR> { f32x4 va[NR], vb[NR], ka[NR], kb[NR];
  __device__ __forceinline__ void ldv(int i, const float* p) { va[i] = *(const f32x4*)p; vb[i] = *(const f32x4*)(p + 4); }
  __device__ __forceinline__ void ldk(int i, const float* p) { ka[i] = *(const f32x4*)p; kb[i] = *(const f32x4*)(p + 4); }
  __device__ __forceinline__ bf16x8 gv(int i) const { return pack8(va[i], vb[i]); } __device__ __forceinline__ bf16x8 gk(int i) const { return pack8(ka[i], kb[i]); } };
__device__ __forceinline__ int voff(int row, int sc) { return (row >> 5) * 8192 + 2 * ((sc >> 4) * 512 + (row & 31) * 16 + (sc & 8)); }

__device__ __forceinline__ void partialSM(f32x16& p0, f32x16& p1, float& m_reg, float& msub, float& alpha, int kbase, int lim, int lim0) {
  if constexpr (CAUSAL || HAS_LENS) { if (kbase + KVBLK - 1 > lim0) {
    const float ninf = -__builtin_inff(); const int hi = (threadIdx.x & 63) >> 5;
    for (int r = 0; r < 16; ++r) { const int k = kbase + crow(r, hi); p0[r] = k > lim ? ninf : p0[r]; p1[r] = k + 32 > lim ? ninf : p1[r]; } } }
  float pmax = p0[0]; for (int r = 1; r < 16; ++r) pmax = fmaxf(pmax, p0[r]); for (int r = 0; r < 16; ++r) pmax = fmaxf(pmax, p1[r]);
  { auto rr = __builtin_amdgcn_permlane32_swap(__float_as_uint(pmax), __float_as_uint(pmax), false, false);
    pmax = fmaxf(__uint_as_float(rr[0]), __uint_as_float(rr[1])); }
  pmax *= SL2E;
  if (__builtin_expect(__all(pmax - m_reg <= THR2), 1)) { alpha = 1.f; }
  else { float mn = fmaxf(m_reg, pmax); msub = (mn == -__builtin_inff()) ? 0.f : mn; alpha = __builtin_amdgcn_exp2f(m_reg - msub); m_reg = mn; }
  float nm = -msub;
  for (int r = 0; r < 16; ++r) p0[r] = fmaf(p0[r], SL2E, nm); for (int r = 0; r < 16; ++r) p1[r] = fmaf(p1[r], SL2E, nm);
  for (int r = 0; r < 16; ++r) p0[r] = __builtin_amdgcn_exp2f(p0[r]);
}
__device__ __forceinline__ void finishSM(f32x16& p0, f32x16& p1, float alpha, float& l_reg, bf16x8& pa0, bf16x8& pa1, bf16x8& pa2, bf16x8& pa3) {
  for (int r = 0; r < 16; ++r) p1[r] = __builtin_amdgcn_exp2f(p1[r]);
  float ps = 0; for (int r = 0; r < 16; ++r) ps += p0[r]; for (int r = 0; r < 16; ++r) ps += p1[r];
  { auto rr = __builtin_amdgcn_permlane32_swap(__float_as_uint(ps), __float_as_uint(ps), false, false); ps = __uint_as_float(rr[0]) + __uint_as_float(rr[1]); }
  l_reg = l_reg * alpha + ps;
#define PK4(P, BASE, OUT) do { unsigned a0 = cvtpk(P[BASE + 0], P[BASE + 1]), a1 = cvtpk(P[BASE + 2], P[BASE + 3]); \
    unsigned b0 = cvtpk(P[BASE + 4], P[BASE + 5]), b1 = cvtpk(P[BASE + 6], P[BASE + 7]); \
    auto r0 = __builtin_amdgcn_permlane32_swap(a0, b0, false, false); auto r1 = __builtin_amdgcn_permlane32_swap(a1, b1, false, false); \
    u32x4 w = {r0[0], r1[0], r0[1], r1[1]}; OUT = *reinterpret_cast<bf16x8*>(&w); } while (0)
  PK4(p0, 0, pa0); PK4(p0, 8, pa1); PK4(p1, 0, pa2); PK4(p1, 8, pa3);
#undef PK4
}
template <int QP, int NWT> __device__ __forceinline__ void qkt(f32x16& p0, f32x16& p1, const bf16* Ks, const bf16x8* qr, const char* qp, int r32, int hi) {
  p0 = f32x16{}; p1 = f32x16{};
#pragma unroll
  for (int d0 = 0; d0 < 8; ++d0) { int cb = (d0 * 16 + hi * 8) * 2;
    bf16x8 b0 = *reinterpret_cast<const bf16x8*>((const char*)Ks + KSWZ(r32, cb));
    bf16x8 b1 = *reinterpret_cast<const bf16x8*>((const char*)Ks + KSWZ(32 + r32, cb));
    const bf16x8 q = d0 < 8 - QP ? qr[d0] : *reinterpret_cast<const bf16x8*>(qp + (d0 - (8 - QP)) * (64 * NWT * 16));
    p0 = __builtin_amdgcn_mfma_f32_32x32x16_bf16(b0, q, p0, 0, 0, 0);
    p1 = __builtin_amdgcn_mfma_f32_32x32x16_bf16(b1, q, p1, 0, 0, 0); }
}
__device__ __forceinline__ void pv_d0(f32x16* o, int vb, bf16x8 pa0, bf16x8 pa1, bf16x8 pa2, bf16x8 pa3) {
#pragma unroll
  for (int d0 = 0; d0 < 4; ++d0) { s16x4 lo[4], hi[4];
#pragma unroll
    for (int ks = 0; ks < 4; ++ks) { int a = vb + d0 * 2048 + (ks >> 1) * 8192 + (ks & 1) * 512;
      asm volatile("ds_read_b64_tr_b16 %0,%1" : "=&v"(lo[ks]) : "v"(a) : "memory");
      asm volatile("ds_read_b64_tr_b16 %0,%1 offset:128" : "=&v"(hi[ks]) : "v"(a) : "memory"); }
    asm volatile("s_waitcnt lgkmcnt(0)" ::: "memory"); SBAR();
#define PK(k) (bf16x8){lo[k][0], lo[k][1], lo[k][2], lo[k][3], hi[k][0], hi[k][1], hi[k][2], hi[k][3]}
    o[d0] = __builtin_amdgcn_mfma_f32_32x32x16_bf16(pa0, PK(0), o[d0], 0, 0, 0);
    o[d0] = __builtin_amdgcn_mfma_f32_32x32x16_bf16(pa1, PK(1), o[d0], 0, 0, 0);
    o[d0] = __builtin_amdgcn_mfma_f32_32x32x16_bf16(pa2, PK(2), o[d0], 0, 0, 0);
    o[d0] = __builtin_amdgcn_mfma_f32_32x32x16_bf16(pa3, PK(3), o[d0], 0, 0, 0);
#undef PK
  }
}

template <class TQ, class TKV>
__global__ __launch_bounds__(64 * InCfg<TKV>::NWT, NW / InCfg<TKV>::NWT)
void gqa_chunk_fwd(const TQ* __restrict__ Q, const TKV* __restrict__ K, const TKV* __restrict__ V, const int* __restrict__ lens, float* __restrict__ O, float* __restrict__ pml, float* __restrict__ po) {
  constexpr int NWT = InCfg<TKV>::NWT, NS = NSPLIT * (NW / NWT), TPS = NT / NS;
  constexpr int NR = KVBLK / (4 * NWT);
  static_assert(ROWS <= QBLK * NWT, "rows must fit this form's waves");
  const int tid = threadIdx.x, wid = tid >> 6, lane = tid & 63, r32 = lane & 31, hi = lane >> 5;
  const int split = blockIdx.x, kvh = blockIdx.y, b = blockIdx.z;
  const TQ* Qg = Q + ((size_t)b * HQ + (size_t)kvh * G) * SQ * D;
  const TKV* Kh = K + (((size_t)b * HKV + kvh) * SK + (size_t)split * TPS * KVBLK) * D;
  const TKV* Vh = V + (((size_t)b * HKV + kvh) * SK + (size_t)split * TPS * KVBLK) * D;
  extern __shared__ __attribute__((aligned(16))) char shm[];
  bf16* V_lds = (bf16*)shm; bf16* K_lds = (bf16*)(shm + 2 * SHM_V);
  float* ws = (float*)(shm + SHM_WS) + wid * 64; float* li_l = ws; float* al_l = ws + 32;
  constexpr int QP = InCfg<TKV>::QP; char* qp = shm + shm_qp<NWT>() + tid * 16;
  const int R = wid * QBLK + r32, Rc = R < ROWS ? R : ROWS - 1;
  int len = SK; if constexpr (HAS_LENS) { len = __builtin_amdgcn_readfirstlane(lens[b]); len = len < 0 ? 0 : (len > SK ? SK : len); }
  const int lim0 = CAUSAL ? len - SQ : len - 1, lim = CAUSAL ? lim0 + Rc % SQ : lim0, kb0 = split * TPS * KVBLK;
  bf16x8 qr[8]; const TQ* qrow = Qg + (size_t)Rc * D;
  float m_reg = -__builtin_inff(), msub = 0.f, l_reg = 0.f; f32x16 o[4] = {};
  const int sr = tid >> 4, sc = (tid & 15) * 8;
  const int vb0 = (int)(uintptr_t)V_lds + (lane & 15) * 8 + ((lane >> 4) & 1) * 1024 + (lane >> 5) * 256;
  Stage<TKV, NR> st; int sk0 = 0; (void)sk0;
#define SLOAD(k0) do { sk0 = kb0 + (k0); \
    if constexpr (NR == 2) st.load(Vh + (size_t)((k0) + sr) * D + sc, Vh + (size_t)((k0) + 32 + sr) * D + sc, Kh + (size_t)((k0) + sr) * D + sc, Kh + (size_t)((k0) + 32 + sr) * D + sc); \
    else { const TKV* vr_ = Vh + (size_t)((k0) + NR * sr) * D + sc; const TKV* kr_ = Kh + (size_t)((k0) + NR * sr) * D + sc; \
           for (int i_ = 0; i_ < NR; ++i_) st.ldv(i_, vr_ + i_ * D); for (int i_ = 0; i_ < NR; ++i_) st.ldk(i_, kr_ + i_ * D); } } while (0)
#define SWRITE(buf) do { if constexpr (NR == 2) { bf16x8 v0_ = st.gv0(), v1_ = st.gv1(), k0_ = st.gk0(), k1_ = st.gk1(); \
      if constexpr (HAS_LENS) { if (sk0 + KVBLK > len) { const bf16x8 z_ = {}; if (sk0 + sr >= len) { v0_ = z_; k0_ = z_; } if (sk0 + 32 + sr >= len) { v1_ = z_; k1_ = z_; } } } \
      *(bf16x8*)((char*)V_lds + (buf) * SHM_V + voff(sr, sc)) = v0_; *(bf16x8*)((char*)V_lds + (buf) * SHM_V + voff(32 + sr, sc)) = v1_; int kc = sc * 2; \
      *(bf16x8*)((char*)K_lds + (buf) * SHM_K + KSWZ(sr, kc)) = k0_; *(bf16x8*)((char*)K_lds + (buf) * SHM_K + KSWZ(32 + sr, kc)) = k1_; } \
    else { for (int i_ = 0; i_ < NR; ++i_) { const int row_ = NR * sr + i_; bf16x8 v_ = st.gv(i_), k_ = st.gk(i_); \
      if constexpr (HAS_LENS) { if (sk0 + KVBLK > len) { if (sk0 + row_ >= len) { v_ = bf16x8{}; k_ = bf16x8{}; } } } \
      *(bf16x8*)((char*)V_lds + (buf) * SHM_V + voff(row_, sc)) = v_; *(bf16x8*)((char*)K_lds + (buf) * SHM_K + KSWZ(row_, sc * 2)) = k_; } } } while (0)
#define RESC(a) do { if (__any((a) < 1.f)) { if constexpr (NWT == NW || !XCHG4) { if (hi == 0) al_l[r32] = (a); asm volatile("s_waitcnt lgkmcnt(0)" ::: "memory"); \
      for (int d = 0; d < 4; ++d) for (int r = 0; r < 16; ++r) o[d][r] *= al_l[crow(r, hi)]; } \
    else { const int ai_ = __float_as_int(a);    \
      for (int r = 0; r < 16; ++r) { const float a0_ = __int_as_float(__builtin_amdgcn_readlane(ai_, crow(r, 0))), a1_ = __int_as_float(__builtin_amdgcn_readlane(ai_, crow(r, 1))); \
        const float ar_ = hi ? a1_ : a0_; for (int d = 0; d < 4; ++d) o[d][r] *= ar_; } } } } while (0)
  f32x16 pA0, pA1, pB0, pB1; float alA, alB; bf16x8 pa0, pa1, pa2, pa3;
  for (int d0 = 0; d0 < 8; ++d0) qr[d0] = ld8(qrow + d0 * 16 + hi * 8);
  for (int j = 0; j < QP; ++j) *reinterpret_cast<bf16x8*>(qp + j * (64 * NWT * 16)) = qr[8 - QP + j];
  SLOAD(0); SWRITE(0); __syncthreads();
  qkt<QP, NWT>(pA0, pA1, K_lds, qr, qp, r32, hi); partialSM(pA0, pA1, m_reg, msub, alA, kb0, lim, lim0);
  SLOAD(KVBLK); SWRITE(1); __syncthreads();
  for (int j = 1; j + 1 < TPS; j += 2) {
    SBAR(); qkt<QP, NWT>(pB0, pB1, (bf16*)((char*)K_lds + SHM_K), qr, qp, r32, hi);
    finishSM(pA0, pA1, alA, l_reg, pa0, pa1, pa2, pa3); SBAR();
    SLOAD((j + 1) * KVBLK); SBAR();
    pv_d0(o, vb0, pa0, pa1, pa2, pa3); partialSM(pB0, pB1, m_reg, msub, alB, kb0 + j * KVBLK, lim, lim0);
    __syncthreads(); asm volatile("s_waitcnt vmcnt(0)" ::: "memory"); SWRITE(0);
    RESC(alB); __syncthreads();
    SBAR(); qkt<QP, NWT>(pA0, pA1, K_lds, qr, qp, r32, hi);
    finishSM(pB0, pB1, alB, l_reg, pa0, pa1, pa2, pa3); SBAR();
    if (j + 2 < TPS) { SLOAD((j + 2) * KVBLK); SBAR(); }
    pv_d0(o, vb0 + (int)SHM_V, pa0, pa1, pa2, pa3); partialSM(pA0, pA1, m_reg, msub, alA, kb0 + (j + 1) * KVBLK, lim, lim0);
    __syncthreads(); if (j + 2 < TPS) { asm volatile("s_waitcnt vmcnt(0)" ::: "memory"); SWRITE(1); }
    RESC(alA); __syncthreads();
  }
  SBAR(); qkt<QP, NWT>(pB0, pB1, (bf16*)((char*)K_lds + SHM_K), qr, qp, r32, hi);
  finishSM(pA0, pA1, alA, l_reg, pa0, pa1, pa2, pa3); SBAR();
  pv_d0(o, vb0, pa0, pa1, pa2, pa3); partialSM(pB0, pB1, m_reg, msub, alB, kb0 + (TPS - 1) * KVBLK, lim, lim0);
  __syncthreads(); RESC(alB);
  finishSM(pB0, pB1, alB, l_reg, pa0, pa1, pa2, pa3); SBAR();
  pv_d0(o, vb0 + (int)SHM_V, pa0, pa1, pa2, pa3);
#undef SLOAD
#undef SWRITE
#undef RESC
  if constexpr (NS == 1) {
    float rli[16];
    if constexpr (NWT == NW || !XCHG4) { if (hi == 0) li_l[r32] = l_reg; asm volatile("s_waitcnt lgkmcnt(0)" ::: "memory");
#pragma unroll
      for (int r = 0; r < 16; ++r) { float l = li_l[crow(r, hi)]; rli[r] = l > 0.f ? __builtin_amdgcn_rcpf(l) : 0.f; } }
    else {
#pragma unroll
      for (int r = 0; r < 16; ++r) { const int li_ = __float_as_int(l_reg); const float l0 = __int_as_float(__builtin_amdgcn_readlane(li_, crow(r, 0))), l1 = __int_as_float(__builtin_amdgcn_readlane(li_, crow(r, 1)));
        const float l = hi ? l1 : l0; rli[r] = l > 0.f ? __builtin_amdgcn_rcpf(l) : 0.f; } }
#pragma unroll
    for (int r = 0; r < 16; ++r) { const int Ro = wid * QBLK + crow(r, hi); if (Ro < ROWS) { float* orow = O + (((size_t)b * HQ + (size_t)kvh * G) * SQ + Ro) * D;
        for (int d0 = 0; d0 < 4; ++d0) orow[d0 * 32 + r32] = o[d0][r] * rli[r]; } }
  } else {
    const size_t row0 = ((size_t)b * HQ + (size_t)kvh * G) * SQ;
    if (hi == 0 && R < ROWS) { float* p = pml + ((row0 + R) * NS + split) * 2; p[0] = m_reg; p[1] = l_reg; }
#pragma unroll
    for (int r = 0; r < 16; ++r) { const int Ro = wid * QBLK + crow(r, hi); if (Ro < ROWS) { float* prow = po + ((row0 + Ro) * NS + split) * D;
        for (int d0 = 0; d0 < 4; ++d0) prow[d0 * 32 + r32] = o[d0][r]; } }
  }
}

constexpr int CMB_ROWS = 8, CMB_BLOCKS = (int)(((size_t)B * HQ * SQ + CMB_ROWS - 1) / CMB_ROWS);
template <int NS> __global__ __launch_bounds__(256) void gqa_chunk_combine(const float* __restrict__ pml, const float* __restrict__ po, float* __restrict__ O) {
  const size_t row = (size_t)blockIdx.x * CMB_ROWS + (threadIdx.x >> 5); const int d = (threadIdx.x & 31) * 4;
  if (row >= (size_t)B * HQ * SQ) return;
  const float* ml = pml + row * NS * 2; const float* acc = po + row * NS * D + d;
  float M = -__builtin_inff(); for (int s = 0; s < NS; ++s) M = fmaxf(M, ml[2 * s]);
  const float Msub = (M == -__builtin_inff()) ? 0.f : M; float L = 0.f; f32x4 o = {0.f, 0.f, 0.f, 0.f};
  for (int s = 0; s < NS; ++s) { const float m = ml[2 * s], l = ml[2 * s + 1]; const float w = l > 0.f ? __builtin_amdgcn_exp2f(m - Msub) : 0.f;
    const f32x4 a = *reinterpret_cast<const f32x4*>(acc + (size_t)s * D); L += w * l; o[0] += w * a[0]; o[1] += w * a[1]; o[2] += w * a[2]; o[3] += w * a[3]; }
  const float inv = L > 0.f ? 1.f / L : 0.f; o[0] *= inv; o[1] *= inv; o[2] *= inv; o[3] *= inv;
  *reinterpret_cast<f32x4*>(O + row * D + d) = o;
}

__global__ __launch_bounds__(256) void cvt_f32_bf16(const float* __restrict__ q, const float* __restrict__ k, const float* __restrict__ v, bf16* __restrict__ qb, bf16* __restrict__ kb, bf16* __restrict__ vb) {
  const size_t i0 = ((size_t)blockIdx.x * 256 + threadIdx.x) * 8, step = (size_t)gridDim.x * 256 * 8;
  for (size_t i = i0; i < NQ; i += step) *reinterpret_cast<bf16x8*>(qb + i) = ld8(q + i);
  for (size_t i = i0; i < NKV; i += step) { *reinterpret_cast<bf16x8*>(kb + i) = ld8(k + i); *reinterpret_cast<bf16x8*>(vb + i) = ld8(v + i); }
}
template <class TQ, class TKV>
static void gqa_chunk_forward(const TQ* q, const TKV* k, const TKV* v, const int* lens, float* o, void* ws, hipStream_t stream) {
  int lds_set = 0;
  constexpr size_t shm = shm_bytes<TKV>();
  if (lds_set == 0) { lds_set = hipFuncSetAttribute((const void*)gqa_chunk_fwd<TQ, TKV>, hipFuncAttributeMaxDynamicSharedMemorySize, (int)shm) == hipSuccess ? 1 : -1; if (lds_set < 0) fprintf(stderr, "kernel_launch: hipFuncSetAttribute(%zu B LDS) failed\n", shm); }
  if (lds_set < 0) return;
  float* pml = (float*)((char*)ws + WS_PML); float* po = (float*)((char*)ws + WS_PO);
  constexpr int NS = ns_for<TKV>();
  hipLaunchKernelGGL((gqa_chunk_fwd<TQ, TKV>), dim3(NS, HKV, B), dim3(64 * nw_for<TKV>()), shm, stream, q, k, v, lens, o, pml, po);
  if constexpr (NS > 1) hipLaunchKernelGGL((gqa_chunk_combine<NS>), dim3(CMB_BLOCKS), dim3(256), 0, stream, (const float*)pml, (const float*)po, o);
  const hipError_t le = hipPeekAtLastError();
  if (le != hipSuccess) fprintf(stderr, "kernel_launch: launch failed: %s (grid %d x %d x %d, %zu B LDS)\n", hipGetErrorName(le), NS, HKV, B, shm);
}

template<int MODE>
__global__ __launch_bounds__(256) void proj(const void* __restrict__ Ain, const unsigned short* __restrict__ W, unsigned short* __restrict__ out) {
  constexpr int KK = MODE ? 4096 : 1024, NN = MODE ? 1024 : 4096;
  const int lane = threadIdx.x & 63, w = threadIdx.x >> 6, r = lane & 31, h = lane >> 5;
  __shared__ float red[4][16][64];
  const int tile = blockIdx.x, nt = tile % (NN / 32), mt = tile / (NN / 32);
  f32x16 acc = {};
  const int tok = mt * 32 + r;
  for (int ks = w * (KK / 64); ks < (w + 1) * (KK / 64); ++ks) {
    const int k0 = ks * 16 + 8 * h;
    bf16x8 a, b;
    if (MODE == 0) a = *(const bf16x8*)((const unsigned short*)Ain + (size_t)tok * 1024 + k0);
    else a = ld8((const float*)Ain + (((size_t)(tok >> 4) * 32 + (k0 >> 7)) * 16 + (tok & 15)) * 128 + (k0 & 127));
    for (int j = 0; j < 8; ++j) b[j] = (short)W[(size_t)(k0 + j) * NN + nt * 32 + r];
    acc = __builtin_amdgcn_mfma_f32_32x32x16_bf16(a, b, acc, 0, 0, 0);
  }
  for (int i = 0; i < 16; ++i) red[w][i][lane] = acc[i];
  __syncthreads();
  for (int i = w * 4; i < w * 4 + 4; ++i) {
    float v = red[0][i][lane] + red[1][i][lane] + red[2][i][lane] + red[3][i][lane];
    const int t = mt * 32 + crow(i, h), n = nt * 32 + r;
    unsigned short o = (unsigned short)(cvtpk(v, v) & 0xffff);
    if (MODE == 0) out[(((size_t)(t >> 4) * 32 + (n >> 7)) * 16 + (t & 15)) * 128 + (n & 127)] = o;
    else out[(size_t)t * 1024 + n] = o;
  }
}
#define PROBE 0
__global__ __launch_bounds__(512) void rd(const u32x4* __restrict__ k, const u32x4* __restrict__ v, unsigned* __restrict__ out) {
  const size_t per = 16384;
  const u32x4* kp = k + blockIdx.x*per + threadIdx.x;
  const u32x4* vp = v + blockIdx.x*per + threadIdx.x;
  u32x4 acc = {0,0,0,0};
  #pragma unroll 1
  for (int i = 0; i < 32; i += 4) {
    u32x4 a[4], b[4];
    #pragma unroll
    for (int j = 0; j < 4; ++j) { if (PROBE==2) { a[j] = __builtin_nontemporal_load(kp + (i+j)*512); b[j] = __builtin_nontemporal_load(vp + (i+j)*512);} else { a[j] = kp[(i+j)*512]; b[j] = vp[(i+j)*512]; } }
    #pragma unroll
    for (int j = 0; j < 4; ++j) { acc ^= a[j]; acc ^= b[j]; }
  }
  out[blockIdx.x*512 + threadIdx.x] = acc[0]^acc[1]^acc[2]^acc[3];
}
extern "C" void kernel_launch(void* const* d_in, const int* in_sizes, int n_in, void* d_out, int out_size, void* d_ws, size_t ws_size, hipStream_t stream) {
  char* ws = (char*)d_ws;
  unsigned short* q = (unsigned short*)(ws + (64 << 20)); float* o = (float*)(ws + (96 << 20));
  if (PROBE) rd<<<256, 512, 0, stream>>>((const u32x4*)d_in[1], (const u32x4*)d_in[2], (unsigned*)(ws + (128 << 20)));
  proj<0><<<16 * 128, 256, 0, stream>>>(d_in[0], (const unsigned short*)d_in[3], q);
  gqa_chunk_forward<bf16, bf16>((const bf16*)q, (const bf16*)d_in[1], (const bf16*)d_in[2], nullptr, o, d_ws, stream);
  proj<1><<<16 * 32, 256, 0, stream>>>(o, (const unsigned short*)d_in[4], (unsigned short*)d_out);
}
```

```cpp
#include <hip/hip_runtime.h>

__global__ __launch_bounds__(256, 1) void gqa_fused(const void* x, const void* k, const void* v, const void* wq, const void* wo,
                                                    void* out, void* ws) {
  unsigned tid = threadIdx.x;
  asm volatile(
      ".Lgq_start:\n"
      "s_mov_b64 s[16:17], %0\n"
      "s_mov_b32 s18, %1\n"
      "s_load_dwordx8 s[56:63], s[16:17], 0\n"
      "s_load_dwordx4 s[64:67], s[16:17], 32\n"
      "s_load_dwordx2 s[68:69], s[16:17], 48\n"
      "v_and_b32 v172, 63, %2\n"
      "v_readfirstlane_b32 s19, %2\n"
      "s_lshr_b32 s80, s18, 3\n"
      "s_nop 0\n"
      "s_lshr_b32 s19, s19, 6\n"
      "s_getpc_b64 s[54:55]\n"
      ".Lgq_pc:\n"
      "s_lshl_b32 s80, s80, 2\n"
      "s_add_u32 s80, s80, s19\n"
      "s_lshl_b32 s80, s80, 8\n"
      "v_and_b32 v1, 1, v172\n"
      "v_lshlrev_b32 v1, 7, v1\n"
      "v_add_u32 v1, s80, v1\n"
      "v_min_u32 v1, .Lgq_end-.Lgq_pc-4, v1\n"
      "global_load_dword v15, v1, s[54:55]\n"
      "s_lshl_b32 s71, s19, 12\n"
      "v_lshrrev_b32 v1, 5, v172\n"
      "v_bfe_u32 v2, v172, 4, 1\n"
      "v_and_b32 v3, 3, v172\n"
      "v_xor_b32 v3, v3, v2\n"
      "v_lshlrev_b32 v3, 4, v3\n"
      "v_lshl_add_u32 v3, v1, 6, v3\n"
      "v_xor_b32 v4, 32, v3\n"
      "v_bfe_u32 v5, v172, 2, 3\n"
      "v_lshl_add_u32 v5, s19, 4, v5\n"
      "v_lshl_add_u32 v6, v5, 11, v3\n"
      "v_add_u32 v162, 0x400, v6\n"
      "v_add_u32 v163, 128, v6\n"
      "v_lshl_add_u32 v6, v5, 11, v4\n"
      "v_add_u32 v164, 15360, v6\n"
      "v_add_u32 v165, 14464, v6\n"
      "v_lshl_add_u32 v6, v5, 13, v3\n"
      "v_add_u32 v166, 0x400, v6\n"
      "v_add_u32 v167, 256, v6\n"
      "v_lshl_add_u32 v6, v5, 13, v4\n"
      "v_add_u32 v168, 64512, v6\n"
      "v_add_u32 v169, 63744, v6\n"
      "s_bfe_u32 s97, s18, 0x10003\n"
      "s_xor_b32 s97, s97, 1\n"
      "s_bfe_u32 s80, s18, 0x30004\n"
      "s_bfe_u32 s81, s18, 0x10007\n"
      "s_mov_b32 s85, s97\n"
      "s_lshl_b32 s85, s85, 1\n"
      "s_add_u32 s81, s81, s85\n"
      "s_add_u32 s89, s80, s81\n"
      "s_and_b32 s89, s89, 1\n"
      "s_lshl_b32 s89, s89, 2\n"
      "s_bfe_u32 s82, s18, 0x30000\n"
      "s_lshl_b32 s82, s82, 10\n"
      "s_mov_b32 s85, s97\n"
      "s_lshl_b32 s85, s85, 9\n"
      "s_add_u32 s82, s82, s85\n"
      "s_bfe_u32 s85, s18, 0x10007\n"
      "s_lshl_b32 s85, s85, 7\n"
      "s_add_u32 s82, s82, s85\n"
      "s_lshl_b32 s83, s80, 17\n"
      "s_sub_u32 s84, 8389632, s82\n"
      "s_mov_b32 s72, s71\n"
      "s_waitcnt lgkmcnt(0)\n"
      "s_add_u32 s20, s56, s83\n"
      "s_addc_u32 s21, s57, 0\n"
      "s_sub_u32 s20, s20, 1024\n"
      "s_subb_u32 s21, s21, 0\n"
      "s_and_b32 s21, s21, 0xffff\n"
      "s_mov_b32 s22, 132096\n"
      "s_mov_b32 s23, 0x20000\n"
      "s_add_u32 s24, s62, s82\n"
      "s_addc_u32 s25, s63, 0\n"
      "s_sub_u32 s24, s24, 1024\n"
      "s_subb_u32 s25, s25, 0\n"
      "s_and_b32 s25, s25, 0xffff\n"
      "s_mov_b32 s26, s84\n"
      "s_mov_b32 s27, 0x20000\n"
      "s_load_dwordx2 s[48:49], s[68:69], 0\n"
      "s_lshl_b32 s80, s89, 8\n"
      "s_mov_b32 m0, s72\n"
      "s_lshl_b32 s81, s89, 20\n"
      "buffer_load_dwordx4 v162, s[20:23], s80 offen offset:0  lds\n"
      "buffer_load_dwordx4 v163, s[20:23], s80 offen offset:1024  lds\n"
      "buffer_load_dwordx4 v164, s[20:23], s80 offen offset:2048  lds\n"
      "buffer_load_dwordx4 v165, s[20:23], s80 offen offset:3072  lds\n"
      "s_add_u32 m0, s72, 16384\n"
      "s_add_u32 s82, s81, 0x80000\n"
      "buffer_load_dwordx4 v166, s[24:27], s81 offen offset:0  lds\n"
      "buffer_load_dwordx4 v167, s[24:27], s81 offen offset:1024  lds\n"
      "buffer_load_dwordx4 v168, s[24:27], s81 offen offset:2048  lds\n"
      "buffer_load_dwordx4 v169, s[24:27], s81 offen offset:3072  lds\n"
      "s_add_u32 m0, s72, 32768\n"
      "s_add_u32 s89, s89, 1\n"
      "buffer_load_dwordx4 v166, s[24:27], s82 offen offset:0  lds\n"
      "buffer_load_dwordx4 v167, s[24:27], s82 offen offset:1024  lds\n"
      "buffer_load_dwordx4 v168, s[24:27], s82 offen offset:2048  lds\n"
      "buffer_load_dwordx4 v169, s[24:27], s82 offen offset:3072  lds\n"
      "s_and_b32 s89, s89, 7\n"
      "s_add_u32 s72, s72, 49152\n"
      "s_cmp_ge_u32 s72, 147456\n"
      "s_cselect_b32 s99, 147456, 0\n"
      "s_sub_u32 s72, s72, s99\n"
      "s_mov_b32 s40, s68\n"
      "s_and_b32 s41, s69, 0xffff\n"
      "s_mov_b32 s42, 30408704\n"
      "s_mov_b32 s43, 0x20000\n"
      "s_bfe_u32 s94, s18, 0x30004\n"
      "s_lshl_b32 s94, s94, 20\n"
      "s_bfe_u32 s85, s18, 0x20001\n"
      "s_lshl_b32 s85, s85, 16\n"
      "s_add_u32 s94, s94, s85\n"
      "s_bfe_u32 s85, s18, 0x10000\n"
      "s_lshl_b32 s85, s85, 15\n"
      "s_add_u32 s94, s94, s85\n"
      "s_mov_b32 s85, s97\n"
      "s_lshl_b32 s85, s85, 14\n"
      "s_add_u32 s94, s94, s85\n"
      "s_bfe_u32 s85, s18, 0x10007\n"
      "s_lshl_b32 s85, s85, 13\n"
      "s_add_u32 s94, s94, s85\n"
      "s_add_u32 s94, s94, 1048576\n"
      "s_and_b32 s85, s19, 1\n"
      "s_lshl_b32 s85, s85, 12\n"
      "s_add_u32 s94, s94, s85\n"
      "s_lshr_b32 s85, s19, 1\n"
      "s_lshl_b32 s85, s85, 8\n"
      "s_add_u32 s94, s94, s85\n"
      "s_add_u32 s95, s94, 0x80000\n"
      "v_lshlrev_b32 v6, 18, v2\n"
      "v_and_b32 v7, 15, v172\n"
      "v_lshl_add_u32 v6, v7, 4, v6\n"
      "v_lshl_add_u32 v175, v1, 9, v6\n"
      "v_lshl_add_u32 v6, v5, 8, v3\n"
      "v_add_u32 v158, 0x400, v6\n"
      "v_add_u32 v159, 128, v6\n"
      "v_lshl_add_u32 v6, v5, 8, v4\n"
      "v_add_u32 v160, 1024, v6\n"
      "v_add_u32 v161, 128, v6\n"
      "v_bfe_u32 v3, v172, 3, 2\n"
      "v_lshlrev_b32 v3, 11, v3\n"
      "v_and_b32 v4, 7, v172\n"
      "v_lshlrev_b32 v171, 3, v4\n"
      "v_lshl_add_u32 v3, v4, 6, v3\n"
      "v_bfe_u32 v4, v172, 2, 2\n"
      "v_xor_b32 v5, v1, v4\n"
      "v_lshl_add_u32 v152, v5, 4, v3\n"
      "v_xor_b32 v153, 32, v152\n"
      "v_and_b32 v5, 1, v172\n"
      "v_lshl_add_u32 v5, v2, 1, v5\n"
      "v_bfe_u32 v6, v172, 1, 1\n"
      "v_lshlrev_b32 v6, 3, v6\n"
      "v_lshl_add_u32 v6, v4, 6, v6\n"
      "v_xor_b32 v7, v5, v1\n"
      "v_lshl_add_u32 v7, v7, 4, v6\n"
      "v_lshl_add_u32 v7, v1, 8, v7\n"
      "v_add_u32 v154, 98304, v7\n"
      "v_xor_b32 v7, 32, v7\n"
      "v_add_u32 v155, 100352, v7\n"
      "v_lshlrev_b32 v7, 1, v1\n"
      "v_xor_b32 v7, v5, v7\n"
      "v_lshl_add_u32 v7, v7, 4, v6\n"
      "v_lshl_add_u32 v7, v1, 11, v7\n"
      "s_lshl_b32 s80, s19, 9\n"
      "v_add_u32 v156, s80, v7\n"
      "v_xor_b32 v7, 16, v156\n"
      "v_add_u32 v157, 0x100, v7\n"
      "v_lshlrev_b32 v170, 4, v172\n"
      "v_mov_b32 v16, 0\n"
      "v_mov_b32 v17, 0\n"
      "v_mov_b32 v18, 0\n"
      "v_mov_b32 v19, 0\n"
      "v_mov_b32 v20, 0\n"
      "v_mov_b32 v21, 0\n"
      "v_mov_b32 v22, 0\n"
      "v_mov_b32 v23, 0\n"
      "v_mov_b32 v24, 0\n"
      "v_mov_b32 v25, 0\n"
      "v_mov_b32 v26, 0\n"
      "v_mov_b32 v27, 0\n"
      "v_mov_b32 v28, 0\n"
      "v_mov_b32 v29, 0\n"
      "v_mov_b32 v30, 0\n"
      "v_mov_b32 v31, 0\n"
      "v_mov_b32 v32, 0\n"
      "v_mov_b32 v33, 0\n"
      "v_mov_b32 v34, 0\n"
      "v_mov_b32 v35, 0\n"
      "v_mov_b32 v36, 0\n"
      "v_mov_b32 v37, 0\n"
      "v_mov_b32 v38, 0\n"
      "v_mov_b32 v39, 0\n"
      "v_mov_b32 v40, 0\n"
      "v_mov_b32 v41, 0\n"
      "v_mov_b32 v42, 0\n"
      "v_mov_b32 v43, 0\n"
      "v_mov_b32 v44, 0\n"
      "v_mov_b32 v45, 0\n"
      "v_mov_b32 v46, 0\n"
      "v_mov_b32 v47, 0\n"
      "s_bfe_u32 s96, s18, 0x20001\n"
      "s_lshl_b32 s96, s96, 10\n"
      "s_bfe_u32 s85, s18, 0x30004\n"
      "s_lshl_b32 s85, s85, 7\n"
      "s_add_u32 s96, s96, s85\n"
      "s_bfe_u32 s85, s18, 0x10000\n"
      "s_lshl_b32 s85, s85, 5\n"
      "s_add_u32 s96, s96, s85\n"
      "s_mov_b32 s85, s97\n"
      "s_lshl_b32 s85, s85, 4\n"
      "s_add_u32 s96, s96, s85\n"
      "s_bfe_u32 s85, s18, 0x10007\n"
      "s_lshl_b32 s85, s85, 3\n"
      "s_add_u32 s96, s96, s85\n"
      "s_add_u32 s96, s96, 4096\n"
      "s_bfe_u32 s79, s18, 0x10003\n"
      "s_bfe_u32 s70, s18, 0x20001\n"
      "s_bfe_u32 s80, s18, 0x10000\n"
      "s_lshl_b32 s80, s80, 25\n"
      "s_bfe_u32 s85, s18, 0x40004\n"
      "s_lshl_b32 s85, s85, 21\n"
      "s_add_u32 s80, s80, s85\n"
      "s_bfe_u32 s85, s18, 0x20001\n"
      "s_lshl_b32 s85, s85, 19\n"
      "s_add_u32 s80, s80, s85\n"
      "s_mul_i32 s81, s79, 278528\n"
      "s_add_u32 s80, s80, s81\n"
      "s_mul_i32 s81, s79, 32768\n"
      "s_sub_u32 s81, 279552, s81\n"
      "s_mul_i32 s98, s79, 1\n"
      "s_sub_u32 s98, 14, s98\n"
      "s_add_u32 s28, s58, s80\n"
      "s_addc_u32 s29, s59, 0\n"
      "s_sub_u32 s28, s28, 1024\n"
      "s_subb_u32 s29, s29, 0\n"
      "s_and_b32 s29, s29, 0xffff\n"
      "s_mov_b32 s30, s81\n"
      "s_mov_b32 s31, 0x20000\n"
      "s_add_u32 s32, s60, s80\n"
      "s_addc_u32 s33, s61, 0\n"
      "s_sub_u32 s32, s32, 1024\n"
      "s_subb_u32 s33, s33, 0\n"
      "s_and_b32 s33, s33, 0xffff\n"
      "s_mov_b32 s34, s81\n"
      "s_mov_b32 s35, 0x20000\n"
      "s_bfe_u32 s17, s18, 0x10000\n"
      "s_lshl_b32 s17, s17, 2\n"
      "s_bfe_u32 s85, s18, 0x10007\n"
      "s_lshl_b32 s85, s85, 1\n"
      "s_add_u32 s17, s17, s85\n"
      "s_bfe_u32 s85, s18, 0x10003\n"
      "s_add_u32 s17, s17, s85\n"
      "s_mov_b32 s88, s17\n"
      "s_and_b32 s88, s88, 0\n"
      "s_lshl_b32 s74, s88, 18\n"
      "s_mov_b32 s80, s17\n"
      "s_lshl_b32 s80, s80, 8\n"
      "s_sub_u32 s84, 2098176, s80\n"
      "s_mov_b32 s80, s17\n"
      "s_lshl_b32 s80, s80, 8\n"
      "s_bfe_u32 s85, s18, 0x20001\n"
      "s_lshl_b32 s85, s85, 21\n"
      "s_add_u32 s80, s80, s85\n"
      "s_add_u32 s36, s64, s80\n"
      "s_addc_u32 s37, s65, 0\n"
      "s_sub_u32 s36, s36, 1024\n"
      "s_subb_u32 s37, s37, 0\n"
      "s_and_b32 s37, s37, 0xffff\n"
      "s_mov_b32 s38, s84\n"
      "s_mov_b32 s39, 0x20000\n"
      "s_mov_b32 s44, s66\n"
      "s_and_b32 s45, s67, 0xffff\n"
      "s_mov_b32 s46, 1048576\n"
      "s_mov_b32 s47, 0x20000\n"
      "s_mov_b32 s73, 0\n"
      "s_mov_b32 s77, 0\n"
      "s_mov_b32 s78, 49152\n"
      "s_mov_b32 s92, 0x41000000\n"
      "s_mov_b32 s93, 0x3e0293ee\n"
      "s_mov_b32 s75, 5\n"
      "s_waitcnt lgkmcnt(0)\n"
      "s_add_u32 s48, s48, 1\n"
      "v_mov_b32 v4, s48\n"
      "v_mov_b32 v5, s49\n"
      "v_mov_b32 v67, s48\n"
      "v_mov_b32 v71, s48\n"
      "v_mov_b32 v75, s48\n"
      "v_mov_b32 v79, s48\n"
      "v_mov_b32 v83, s48\n"
      "v_mov_b32 v87, s48\n"
      "s_lshl_b32 s80, s89, 8\n"
      "s_mov_b32 m0, s72\n"
      "s_lshl_b32 s81, s89, 20\n"
      "buffer_load_dwordx4 v162, s[20:23], s80 offen offset:0  lds\n"
      "buffer_load_dwordx4 v163, s[20:23], s80 offen offset:1024  lds\n"
      "buffer_load_dwordx4 v164, s[20:23], s80 offen offset:2048  lds\n"
      "buffer_load_dwordx4 v165, s[20:23], s80 offen offset:3072  lds\n"
      "s_add_u32 m0, s72, 16384\n"
      "s_add_u32 s82, s81, 0x80000\n"
      "buffer_load_dwordx4 v166, s[24:27], s81 offen offset:0  lds\n"
      "buffer_load_dwordx4 v167, s[24:27], s81 offen offset:1024  lds\n"
      "buffer_load_dwordx4 v168, s[24:27], s81 offen offset:2048  lds\n"
      "buffer_load_dwordx4 v169, s[24:27], s81 offen offset:3072  lds\n"
      "s_add_u32 m0, s72, 32768\n"
      "s_add_u32 s89, s89, 1\n"
      "buffer_load_dwordx4 v166, s[24:27], s82 offen offset:0  lds\n"
      "buffer_load_dwordx4 v167, s[24:27], s82 offen offset:1024  lds\n"
      "buffer_load_dwordx4 v168, s[24:27], s82 offen offset:2048  lds\n"
      "buffer_load_dwordx4 v169, s[24:27], s82 offen offset:3072  lds\n"
      "s_and_b32 s89, s89, 7\n"
      "s_add_u32 s72, s72, 49152\n"
      "s_cmp_ge_u32 s72, 147456\n"
      "s_cselect_b32 s99, 147456, 0\n"
      "s_sub_u32 s72, s72, s99\n"
      "s_waitcnt vmcnt(12)\n"
      "s_barrier\n"
      "ds_read_b64_tr_b16 a[32:33], v156 offset:16384\n"
      "ds_read_b64_tr_b16 a[34:35], v157 offset:16384\n"
      "ds_read_b128 a[36:39], v152 offset:0\n"
      "ds_read_b128 a[40:43], v152 offset:8192\n"
      "ds_read_b64_tr_b16 a[44:45], v156 offset:20480\n"
      "ds_read_b64_tr_b16 a[46:47], v157 offset:20480\n"
      "ds_read_b128 a[48:51], v153 offset:0\n"
      "ds_read_b128 a[52:55], v153 offset:8192\n"
      "ds_read_b64_tr_b16 a[56:57], v156 offset:24576\n"
      "ds_read_b64_tr_b16 a[58:59], v157 offset:24576\n"
      "ds_read_b128 a[60:63], v152 offset:512\n"
      "ds_read_b128 a[64:67], v152 offset:8704\n"
      "s_cmp_lg_u32 s19, 0\n"
      "s_cbranch_scc1 .Lgq_qf_nofl\n"
      "s_mov_b64 exec, 1\n"
      "buffer_store_dwordx2 v[4:5], off, s[40:43], s96 sc1\n"
      "s_mov_b64 exec, -1\n"
      ".Lgq_qf_nofl:\n"
      "s_lshl_b32 s80, s89, 8\n"
      "s_mov_b32 m0, s72\n"
      "s_lshl_b32 s81, s89, 20\n"
      "buffer_load_dwordx4 v162, s[20:23], s80 offen offset:0  lds\n"
      "buffer_load_dwordx4 v163, s[20:23], s80 offen offset:1024  lds\n"
      "s_waitcnt lgkmcnt(8)\n"
      "v_mfma_f32_32x32x16_bf16 v[16:31], a[32:35], a[36:39], v[16:31]\n"
      "ds_read_b64_tr_b16 a[68:69], v156 offset:28672\n"
      "ds_read_b64_tr_b16 a[70:71], v157 offset:28672\n"
      "buffer_load_dwordx4 v164, s[20:23], s80 offen offset:2048  lds\n"
      "v_mfma_f32_32x32x16_bf16 v[32:47], a[32:35], a[40:43], v[32:47]\n"
      "ds_read_b128 a[72:75], v153 offset:512\n"
      "ds_read_b128 a[76:79], v153 offset:8704\n"
      "buffer_load_dwordx4 v165, s[20:23], s80 offen offset:3072  lds\n"
      "s_waitcnt lgkmcnt(8)\n"
      "v_mfma_f32_32x32x16_bf16 v[16:31], a[44:47], a[48:51], v[16:31]\n"
      "ds_read_b64_tr_b16 a[32:33], v156 offset:32768\n"
      "ds_read_b64_tr_b16 a[34:35], v157 offset:32768\n"
      "s_add_u32 m0, s72, 16384\n"
      "s_add_u32 s82, s81, 0x80000\n"
      "buffer_load_dwordx4 v166, s[24:27], s81 offen offset:0  lds\n"
      "v_mfma_f32_32x32x16_bf16 v[32:47], a[44:47], a[52:55], v[32:47]\n"
      "ds_read_b128 a[36:39], v152 offset:1024\n"
      "ds_read_b128 a[40:43], v152 offset:9216\n"
      "buffer_load_dwordx4 v167, s[24:27], s81 offen offset:1024  lds\n"
      "s_waitcnt lgkmcnt(8)\n"
      "v_mfma_f32_32x32x16_bf16 v[16:31], a[56:59], a[60:63], v[16:31]\n"
      "ds_read_b64_tr_b16 a[44:45], v156 offset:36864\n"
      "ds_read_b64_tr_b16 a[46:47], v157 offset:36864\n"
      "buffer_load_dwordx4 v168, s[24:27], s81 offen offset:2048  lds\n"
      "v_mfma_f32_32x32x16_bf16 v[32:47], a[56:59], a[64:67], v[32:47]\n"
      "ds_read_b128 a[48:51], v153 offset:1024\n"
      "ds_read_b128 a[52:55], v153 offset:9216\n"
      "buffer_load_dwordx4 v169, s[24:27], s81 offen offset:3072  lds\n"
      "s_waitcnt lgkmcnt(8)\n"
      "v_mfma_f32_32x32x16_bf16 v[16:31], a[68:71], a[72:75], v[16:31]\n"
      "ds_read_b64_tr_b16 a[56:57], v156 offset:40960\n"
      "ds_read_b64_tr_b16 a[58:59], v157 offset:40960\n"
      "s_add_u32 m0, s72, 32768\n"
      "s_add_u32 s89, s89, 1\n"
      "buffer_load_dwordx4 v166, s[24:27], s82 offen offset:0  lds\n"
      "v_mfma_f32_32x32x16_bf16 v[32:47], a[68:71], a[76:79], v[32:47]\n"
      "ds_read_b128 a[60:63], v152 offset:1536\n"
      "ds_read_b128 a[64:67], v152 offset:9728\n"
      "buffer_load_dwordx4 v167, s[24:27], s82 offen offset:1024  lds\n"
      "s_waitcnt lgkmcnt(8)\n"
      "v_mfma_f32_32x32x16_bf16 v[16:31], a[32:35], a[36:39], v[16:31]\n"
      "ds_read_b64_tr_b16 a[68:69], v156 offset:45056\n"
      "ds_read_b64_tr_b16 a[70:71], v157 offset:45056\n"
      "buffer_load_dwordx4 v168, s[24:27], s82 offen offset:2048  lds\n"
      "v_mfma_f32_32x32x16_bf16 v[32:47], a[32:35], a[40:43], v[32:47]\n"
      "ds_read_b128 a[72:75], v153 offset:1536\n"
      "ds_read_b128 a[76:79], v153 offset:9728\n"
      "buffer_load_dwordx4 v169, s[24:27], s82 offen offset:3072  lds\n"
      "s_and_b32 s89, s89, 7\n"
      "s_add_u32 s72, s72, 49152\n"
      "s_cmp_ge_u32 s72, 147456\n"
      "s_cselect_b32 s99, 147456, 0\n"
      "s_sub_u32 s72, s72, s99\n"
      ".Lgq_q_loop:\n"
      "s_waitcnt lgkmcnt(8)\n"
      "v_mfma_f32_32x32x16_bf16 v[16:31], a[44:47], a[48:51], v[16:31]\n"
      "s_cmp_eq_u32 s77, 98304\n"
      "s_cselect_b32 s76, 4294868992, s78\n"
      "s_add_u32 s77, s77, s76\n"
      "v_add_u32 v152, s76, v152\n"
      "v_add_u32 v153, s76, v153\n"
      "v_add_u32 v156, s76, v156\n"
      "v_add_u32 v157, s76, v157\n"
      "v_mfma_f32_32x32x16_bf16 v[32:47], a[44:47], a[52:55], v[32:47]\n"
      "s_waitcnt vmcnt(12) lgkmcnt(0)\n"
      "s_barrier\n"
      "ds_read_b64_tr_b16 a[32:33], v156 offset:16384\n"
      "ds_read_b64_tr_b16 a[34:35], v157 offset:16384\n"
      "ds_read_b128 a[36:39], v152 offset:0\n"
      "ds_read_b128 a[40:43], v152 offset:8192\n"
      "v_mfma_f32_32x32x16_bf16 v[16:31], a[56:59], a[60:63], v[16:31]\n"
      "ds_read_b64_tr_b16 a[44:45], v156 offset:20480\n"
      "ds_read_b64_tr_b16 a[46:47], v157 offset:20480\n"
      "s_lshl_b32 s80, s89, 8\n"
      "s_mov_b32 m0, s72\n"
      "s_lshl_b32 s81, s89, 20\n"
      "buffer_load_dwordx4 v162, s[20:23], s80 offen offset:0  lds\n"
      "v_mfma_f32_32x32x16_bf16 v[32:47], a[56:59], a[64:67], v[32:47]\n"
      "ds_read_b128 a[48:51], v153 offset:0\n"
      "ds_read_b128 a[52:55], v153 offset:8192\n"
      "v_mfma_f32_32x32x16_bf16 v[16:31], a[68:71], a[72:75], v[16:31]\n"
      "ds_read_b64_tr_b16 a[56:57], v156 offset:24576\n"
      "ds_read_b64_tr_b16 a[58:59], v157 offset:24576\n"
      "buffer_load_dwordx4 v163, s[20:23], s80 offen offset:1024  lds\n"
      "v_mfma_f32_32x32x16_bf16 v[32:47], a[68:71], a[76:79], v[32:47]\n"
      "ds_read_b128 a[60:63], v152 offset:512\n"
      "ds_read_b128 a[64:67], v152 offset:8704\n"
      "s_waitcnt lgkmcnt(8)\n"
      "v_mfma_f32_32x32x16_bf16 v[16:31], a[32:35], a[36:39], v[16:31]\n"
      "ds_read_b64_tr_b16 a[68:69], v156 offset:28672\n"
      "ds_read_b64_tr_b16 a[70:71], v157 offset:28672\n"
      "buffer_load_dwordx4 v164, s[20:23], s80 offen offset:2048  lds\n"
      "v_mfma_f32_32x32x16_bf16 v[32:47], a[32:35], a[40:43], v[32:47]\n"
      "ds_read_b128 a[72:75], v153 offset:512\n"
      "ds_read_b128 a[76:79], v153 offset:8704\n"
      "buffer_load_dwordx4 v165, s[20:23], s80 offen offset:3072  lds\n"
      "s_waitcnt lgkmcnt(8)\n"
      "v_mfma_f32_32x32x16_bf16 v[16:31], a[44:47], a[48:51], v[16:31]\n"
      "ds_read_b64_tr_b16 a[32:33], v156 offset:32768\n"
      "ds_read_b64_tr_b16 a[34:35], v157 offset:32768\n"
      "s_add_u32 m0, s72, 16384\n"
      "s_add_u32 s82, s81, 0x80000\n"
      "buffer_load_dwordx4 v166, s[24:27], s81 offen offset:0  lds\n"
      "v_mfma_f32_32x32x16_bf16 v[32:47], a[44:47], a[52:55], v[32:47]\n"
      "ds_read_b128 a[36:39], v152 offset:1024\n"
      "ds_read_b128 a[40:43], v152 offset:9216\n"
      "buffer_load_dwordx4 v167, s[24:27], s81 offen offset:1024  lds\n"
      "s_waitcnt lgkmcnt(8)\n"
      "v_mfma_f32_32x32x16_bf16 v[16:31], a[56:59], a[60:63], v[16:31]\n"
      "ds_read_b64_tr_b16 a[44:45], v156 offset:36864\n"
      "ds_read_b64_tr_b16 a[46:47], v157 offset:36864\n"
      "buffer_load_dwordx4 v168, s[24:27], s81 offen offset:2048  lds\n"
      "v_mfma_f32_32x32x16_bf16 v[32:47], a[56:59], a[64:67], v[32:47]\n"
      "ds_read_b128 a[48:51], v153 offset:1024\n"
      "ds_read_b128 a[52:55], v153 offset:9216\n"
      "buffer_load_dwordx4 v169, s[24:27], s81 offen offset:3072  lds\n"
      "s_waitcnt lgkmcnt(8)\n"
      "v_mfma_f32_32x32x16_bf16 v[16:31], a[68:71], a[72:75], v[16:31]\n"
      "ds_read_b64_tr_b16 a[56:57], v156 offset:40960\n"
      "ds_read_b64_tr_b16 a[58:59], v157 offset:40960\n"
      "s_add_u32 m0, s72, 32768\n"
      "s_add_u32 s89, s89, 1\n"
      "buffer_load_dwordx4 v166, s[24:27], s82 offen offset:0  lds\n"
      "v_mfma_f32_32x32x16_bf16 v[32:47], a[68:71], a[76:79], v[32:47]\n"
      "ds_read_b128 a[60:63], v152 offset:1536\n"
      "ds_read_b128 a[64:67], v152 offset:9728\n"
      "buffer_load_dwordx4 v167, s[24:27], s82 offen offset:1024  lds\n"
      "s_waitcnt lgkmcnt(8)\n"
      "v_mfma_f32_32x32x16_bf16 v[16:31], a[32:35], a[36:39], v[16:31]\n"
      "ds_read_b64_tr_b16 a[68:69], v156 offset:45056\n"
      "ds_read_b64_tr_b16 a[70:71], v157 offset:45056\n"
      "buffer_load_dwordx4 v168, s[24:27], s82 offen offset:2048  lds\n"
      "v_mfma_f32_32x32x16_bf16 v[32:47], a[32:35], a[40:43], v[32:47]\n"
      "ds_read_b128 a[72:75], v153 offset:1536\n"
      "ds_read_b128 a[76:79], v153 offset:9728\n"
      "buffer_load_dwordx4 v169, s[24:27], s82 offen offset:3072  lds\n"
      "s_and_b32 s89, s89, 7\n"
      "s_add_u32 s72, s72, 49152\n"
      "s_cmp_ge_u32 s72, 147456\n"
      "s_cselect_b32 s99, 147456, 0\n"
      "s_sub_u32 s72, s72, s99\n"
      "s_sub_u32 s75, s75, 1\n"
      "s_cmp_lg_u32 s75, 0\n"
      "s_cbranch_scc1 .Lgq_q_loop\n"
      "s_waitcnt lgkmcnt(8)\n"
      "v_mfma_f32_32x32x16_bf16 v[16:31], a[44:47], a[48:51], v[16:31]\n"
      "s_cmp_eq_u32 s77, 98304\n"
      "s_cselect_b32 s76, 4294868992, s78\n"
      "s_add_u32 s77, s77, s76\n"
      "v_add_u32 v152, s76, v152\n"
      "v_add_u32 v153, s76, v153\n"
      "v_add_u32 v156, s76, v156\n"
      "v_add_u32 v157, s76, v157\n"
      "v_mfma_f32_32x32x16_bf16 v[32:47], a[44:47], a[52:55], v[32:47]\n"
      "s_waitcnt vmcnt(12) lgkmcnt(0)\n"
      "s_barrier\n"
      "ds_read_b64_tr_b16 a[32:33], v156 offset:16384\n"
      "ds_read_b64_tr_b16 a[34:35], v157 offset:16384\n"
      "ds_read_b128 a[36:39], v152 offset:0\n"
      "ds_read_b128 a[40:43], v152 offset:8192\n"
      "v_mfma_f32_32x32x16_bf16 v[16:31], a[56:59], a[60:63], v[16:31]\n"
      "ds_read_b64_tr_b16 a[44:45], v156 offset:20480\n"
      "ds_read_b64_tr_b16 a[46:47], v157 offset:20480\n"
      "v_mfma_f32_32x32x16_bf16 v[32:47], a[56:59], a[64:67], v[32:47]\n"
      "ds_read_b128 a[48:51], v153 offset:0\n"
      "ds_read_b128 a[52:55], v153 offset:8192\n"
      "v_mfma_f32_32x32x16_bf16 v[16:31], a[68:71], a[72:75], v[16:31]\n"
      "ds_read_b64_tr_b16 a[56:57], v156 offset:24576\n"
      "ds_read_b64_tr_b16 a[58:59], v157 offset:24576\n"
      "v_mfma_f32_32x32x16_bf16 v[32:47], a[68:71], a[76:79], v[32:47]\n"
      "ds_read_b128 a[60:63], v152 offset:512\n"
      "ds_read_b128 a[64:67], v152 offset:8704\n"
      "s_waitcnt lgkmcnt(8)\n"
      "v_mfma_f32_32x32x16_bf16 v[16:31], a[32:35], a[36:39], v[16:31]\n"
      "ds_read_b64_tr_b16 a[68:69], v156 offset:28672\n"
      "ds_read_b64_tr_b16 a[70:71], v157 offset:28672\n"
      "v_mfma_f32_32x32x16_bf16 v[32:47], a[32:35], a[40:43], v[32:47]\n"
      "ds_read_b128 a[72:75], v153 offset:512\n"
      "ds_read_b128 a[76:79], v153 offset:8704\n"
      "s_waitcnt lgkmcnt(8)\n"
      "v_mfma_f32_32x32x16_bf16 v[16:31], a[44:47], a[48:51], v[16:31]\n"
      "ds_read_b64_tr_b16 a[32:33], v156 offset:32768\n"
      "ds_read_b64_tr_b16 a[34:35], v157 offset:32768\n"
      "v_mfma_f32_32x32x16_bf16 v[32:47], a[44:47], a[52:55], v[32:47]\n"
      "ds_read_b128 a[36:39], v152 offset:1024\n"
      "ds_read_b128 a[40:43], v152 offset:9216\n"
      "s_waitcnt lgkmcnt(8)\n"
      "v_mfma_f32_32x32x16_bf16 v[16:31], a[56:59], a[60:63], v[16:31]\n"
      "ds_read_b64_tr_b16 a[44:45], v156 offset:36864\n"
      "ds_read_b64_tr_b16 a[46:47], v157 offset:36864\n"
      "v_mfma_f32_32x32x16_bf16 v[32:47], a[56:59], a[64:67], v[32:47]\n"
      "ds_read_b128 a[48:51], v153 offset:1024\n"
      "ds_read_b128 a[52:55], v153 offset:9216\n"
      "s_waitcnt lgkmcnt(8)\n"
      "v_mfma_f32_32x32x16_bf16 v[16:31], a[68:71], a[72:75], v[16:31]\n"
      "ds_read_b64_tr_b16 a[56:57], v156 offset:40960\n"
      "ds_read_b64_tr_b16 a[58:59], v157 offset:40960\n"
      "v_mfma_f32_32x32x16_bf16 v[32:47], a[68:71], a[76:79], v[32:47]\n"
      "ds_read_b128 a[60:63], v152 offset:1536\n"
      "ds_read_b128 a[64:67], v152 offset:9728\n"
      "s_waitcnt lgkmcnt(8)\n"
      "v_mfma_f32_32x32x16_bf16 v[16:31], a[32:35], a[36:39], v[16:31]\n"
      "ds_read_b64_tr_b16 a[68:69], v156 offset:45056\n"
      "ds_read_b64_tr_b16 a[70:71], v157 offset:45056\n"
      "v_mfma_f32_32x32x16_bf16 v[32:47], a[32:35], a[40:43], v[32:47]\n"
      "ds_read_b128 a[72:75], v153 offset:1536\n"
      "ds_read_b128 a[76:79], v153 offset:9728\n"
      "s_waitcnt lgkmcnt(8)\n"
      "v_mfma_f32_32x32x16_bf16 v[16:31], a[44:47], a[48:51], v[16:31]\n"
      "s_cmp_eq_u32 s77, 98304\n"
      "s_cselect_b32 s76, 4294868992, s78\n"
      "s_add_u32 s77, s77, s76\n"
      "v_add_u32 v152, s76, v152\n"
      "v_add_u32 v153, s76, v153\n"
      "v_add_u32 v156, s76, v156\n"
      "v_add_u32 v157, s76, v157\n"
      "v_mfma_f32_32x32x16_bf16 v[32:47], a[44:47], a[52:55], v[32:47]\n"
      "s_waitcnt vmcnt(0) lgkmcnt(0)\n"
      "s_barrier\n"
      "ds_read_b64_tr_b16 a[32:33], v156 offset:16384\n"
      "ds_read_b64_tr_b16 a[34:35], v157 offset:16384\n"
      "ds_read_b128 a[36:39], v152 offset:0\n"
      "ds_read_b128 a[40:43], v152 offset:8192\n"
      "v_mfma_f32_32x32x16_bf16 v[16:31], a[56:59], a[60:63], v[16:31]\n"
      "ds_read_b64_tr_b16 a[44:45], v156 offset:20480\n"
      "ds_read_b64_tr_b16 a[46:47], v157 offset:20480\n"
      "s_mov_b32 m0, s72\n"
      "s_nop 0\n"
      "buffer_load_dwordx4 v158, s[28:31], s73 offen offset:0 nt lds\n"
      "v_mfma_f32_32x32x16_bf16 v[32:47], a[56:59], a[64:67], v[32:47]\n"
      "ds_read_b128 a[48:51], v153 offset:0\n"
      "ds_read_b128 a[52:55], v153 offset:8192\n"
      "v_mfma_f32_32x32x16_bf16 v[16:31], a[68:71], a[72:75], v[16:31]\n"
      "ds_read_b64_tr_b16 a[56:57], v156 offset:24576\n"
      "ds_read_b64_tr_b16 a[58:59], v157 offset:24576\n"
      "buffer_load_dwordx4 v159, s[28:31], s73 offen offset:1024 nt lds\n"
      "v_mfma_f32_32x32x16_bf16 v[32:47], a[68:71], a[76:79], v[32:47]\n"
      "ds_read_b128 a[60:63], v152 offset:512\n"
      "ds_read_b128 a[64:67], v152 offset:8704\n"
      "s_waitcnt lgkmcnt(8)\n"
      "v_mfma_f32_32x32x16_bf16 v[16:31], a[32:35], a[36:39], v[16:31]\n"
      "ds_read_b64_tr_b16 a[68:69], v156 offset:28672\n"
      "ds_read_b64_tr_b16 a[70:71], v157 offset:28672\n"
      "buffer_load_dwordx4 v160, s[28:31], s73 offen offset:2048 nt lds\n"
      "v_mfma_f32_32x32x16_bf16 v[32:47], a[32:35], a[40:43], v[32:47]\n"
      "ds_read_b128 a[72:75], v153 offset:512\n"
      "ds_read_b128 a[76:79], v153 offset:8704\n"
      "buffer_load_dwordx4 v161, s[28:31], s73 offen offset:3072 nt lds\n"
      "s_waitcnt lgkmcnt(8)\n"
      "v_mfma_f32_32x32x16_bf16 v[16:31], a[44:47], a[48:51], v[16:31]\n"
      "ds_read_b64_tr_b16 a[32:33], v156 offset:32768\n"
      "ds_read_b64_tr_b16 a[34:35], v157 offset:32768\n"
      "v_mfma_f32_32x32x16_bf16 v[32:47], a[44:47], a[52:55], v[32:47]\n"
      "ds_read_b128 a[36:39], v152 offset:1024\n"
      "ds_read_b128 a[40:43], v152 offset:9216\n"
      "s_waitcnt lgkmcnt(8)\n"
      "v_mfma_f32_32x32x16_bf16 v[16:31], a[56:59], a[60:63], v[16:31]\n"
      "ds_read_b64_tr_b16 a[44:45], v156 offset:36864\n"
      "ds_read_b64_tr_b16 a[46:47], v157 offset:36864\n"
      "v_mfma_f32_32x32x16_bf16 v[32:47], a[56:59], a[64:67], v[32:47]\n"
      "ds_read_b128 a[48:51], v153 offset:1024\n"
      "ds_read_b128 a[52:55], v153 offset:9216\n"
      "s_waitcnt lgkmcnt(8)\n"
      "v_mfma_f32_32x32x16_bf16 v[16:31], a[68:71], a[72:75], v[16:31]\n"
      "ds_read_b64_tr_b16 a[56:57], v156 offset:40960\n"
      "ds_read_b64_tr_b16 a[58:59], v157 offset:40960\n"
      "v_mfma_f32_32x32x16_bf16 v[32:47], a[68:71], a[76:79], v[32:47]\n"
      "ds_read_b128 a[60:63], v152 offset:1536\n"
      "ds_read_b128 a[64:67], v152 offset:9728\n"
      "s_waitcnt lgkmcnt(8)\n"
      "v_mfma_f32_32x32x16_bf16 v[16:31], a[32:35], a[36:39], v[16:31]\n"
      "ds_read_b64_tr_b16 a[68:69], v156 offset:45056\n"
      "ds_read_b64_tr_b16 a[70:71], v157 offset:45056\n"
      "v_mfma_f32_32x32x16_bf16 v[32:47], a[32:35], a[40:43], v[32:47]\n"
      "ds_read_b128 a[72:75], v153 offset:1536\n"
      "ds_read_b128 a[76:79], v153 offset:9728\n"
      "s_waitcnt lgkmcnt(8)\n"
      "v_mfma_f32_32x32x16_bf16 v[16:31], a[44:47], a[48:51], v[16:31]\n"
      "s_cmp_eq_u32 s77, 98304\n"
      "s_cselect_b32 s76, 4294868992, s78\n"
      "s_add_u32 s77, s77, s76\n"
      "v_add_u32 v152, s76, v152\n"
      "v_add_u32 v153, s76, v153\n"
      "v_add_u32 v156, s76, v156\n"
      "v_add_u32 v157, s76, v157\n"
      "v_mfma_f32_32x32x16_bf16 v[32:47], a[44:47], a[52:55], v[32:47]\n"
      "s_waitcnt lgkmcnt(4)\n"
      "v_mfma_f32_32x32x16_bf16 v[16:31], a[56:59], a[60:63], v[16:31]\n"
      "v_mfma_f32_32x32x16_bf16 v[32:47], a[56:59], a[64:67], v[32:47]\n"
      "s_waitcnt lgkmcnt(0)\n"
      "v_mfma_f32_32x32x16_bf16 v[16:31], a[68:71], a[72:75], v[16:31]\n"
      "v_mfma_f32_32x32x16_bf16 v[32:47], a[68:71], a[76:79], v[32:47]\n"
      "s_nop 10\n"
      "v_mul_f32 v16, s93, v16\n"
      "v_mul_f32 v17, s93, v17\n"
      "v_mul_f32 v18, s93, v18\n"
      "v_mul_f32 v19, s93, v19\n"
      "v_mul_f32 v20, s93, v20\n"
      "v_mul_f32 v21, s93, v21\n"
      "v_mul_f32 v22, s93, v22\n"
      "v_mul_f32 v23, s93, v23\n"
      "v_cvt_pk_bf16_f32 v64, v16, v17\n"
      "v_cvt_pk_bf16_f32 v65, v18, v19\n"
      "v_cvt_pk_bf16_f32 v66, v20, v21\n"
      "v_cvt_pk_bf16_f32 v72, v22, v23\n"
      "buffer_store_dwordx4 v[64:67], v175, s[40:43], s94 offen offset:0 sc1\n"
      "v_mul_f32 v24, s93, v24\n"
      "v_mul_f32 v25, s93, v25\n"
      "v_mul_f32 v26, s93, v26\n"
      "v_mul_f32 v27, s93, v27\n"
      "v_mul_f32 v28, s93, v28\n"
      "v_mul_f32 v29, s93, v29\n"
      "v_mul_f32 v30, s93, v30\n"
      "v_mul_f32 v31, s93, v31\n"
      "v_cvt_pk_bf16_f32 v68, v24, v25\n"
      "v_cvt_pk_bf16_f32 v69, v26, v27\n"
      "v_cvt_pk_bf16_f32 v70, v28, v29\n"
      "v_cvt_pk_bf16_f32 v73, v30, v31\n"
      "buffer_store_dwordx4 v[68:71], v175, s[40:43], s94 offen offset:1024 sc1\n"
      "buffer_store_dwordx4 v[72:75], v175, s[40:43], s94 offen offset:2048 sc1\n"
      "v_mul_f32 v32, s93, v32\n"
      "v_mul_f32 v33, s93, v33\n"
      "v_mul_f32 v34, s93, v34\n"
      "v_mul_f32 v35, s93, v35\n"
      "v_mul_f32 v36, s93, v36\n"
      "v_mul_f32 v37, s93, v37\n"
      "v_mul_f32 v38, s93, v38\n"
      "v_mul_f32 v39, s93, v39\n"
      "v_cvt_pk_bf16_f32 v76, v32, v33\n"
      "v_cvt_pk_bf16_f32 v77, v34, v35\n"
      "v_cvt_pk_bf16_f32 v78, v36, v37\n"
      "v_cvt_pk_bf16_f32 v84, v38, v39\n"
      "buffer_store_dwordx4 v[76:79], v175, s[40:43], s95 offen offset:0 sc1\n"
      "v_mul_f32 v40, s93, v40\n"
      "v_mul_f32 v41, s93, v41\n"
      "v_mul_f32 v42, s93, v42\n"
      "v_mul_f32 v43, s93, v43\n"
      "v_mul_f32 v44, s93, v44\n"
      "v_mul_f32 v45, s93, v45\n"
      "v_mul_f32 v46, s93, v46\n"
      "v_mul_f32 v47, s93, v47\n"
      "v_cvt_pk_bf16_f32 v80, v40, v41\n"
      "v_cvt_pk_bf16_f32 v81, v42, v43\n"
      "v_cvt_pk_bf16_f32 v82, v44, v45\n"
      "v_cvt_pk_bf16_f32 v85, v46, v47\n"
      "buffer_store_dwordx4 v[80:83], v175, s[40:43], s95 offen offset:1024 sc1\n"
      "buffer_store_dwordx4 v[84:87], v175, s[40:43], s95 offen offset:2048 sc1\n"
      ".Lgq_q_pub:\n"
      "s_add_u32 m0, s72, 16384\n"
      "s_nop 0\n"
      "buffer_load_dwordx4 v162, s[36:39], s74 offen offset:0  lds\n"
      "buffer_load_dwordx4 v163, s[36:39], s74 offen offset:1024  lds\n"
      "buffer_load_dwordx4 v164, s[36:39], s74 offen offset:2048  lds\n"
      "buffer_load_dwordx4 v165, s[36:39], s74 offen offset:3072  lds\n"
      "s_add_u32 m0, s72, 32768\n"
      "s_nop 0\n"
      "buffer_load_dwordx4 v158, s[32:35], s73 offen offset:0 nt lds\n"
      "buffer_load_dwordx4 v159, s[32:35], s73 offen offset:1024 nt lds\n"
      "buffer_load_dwordx4 v160, s[32:35], s73 offen offset:2048 nt lds\n"
      "buffer_load_dwordx4 v161, s[32:35], s73 offen offset:3072 nt lds\n"
      "s_add_u32 s73, s73, 16384\n"
      "s_add_u32 s74, s74, 0x20000\n"
      "s_and_b32 s74, s74, 0x1fffff\n"
      "s_add_u32 s72, s72, 49152\n"
      "s_cmp_ge_u32 s72, 147456\n"
      "s_cselect_b32 s99, 147456, 0\n"
      "s_sub_u32 s72, s72, s99\n"
      "s_barrier\n"
      "s_mov_b32 m0, s72\n"
      "s_nop 0\n"
      "buffer_load_dwordx4 v158, s[28:31], s73 offen offset:0 nt lds\n"
      "v_lshrrev_b32 v6, 4, v172\n"
      "v_lshlrev_b32 v7, 1, v6\n"
      "v_xor_b32 v7, v7, v172\n"
      "v_and_b32 v7, 3, v7\n"
      "v_lshlrev_b32 v7, 4, v7\n"
      "v_lshl_add_u32 v7, v6, 11, v7\n"
      "v_bfe_u32 v6, v172, 2, 2\n"
      "v_lshl_add_u32 v7, v6, 6, v7\n"
      "v_lshl_add_u32 v7, s19, 9, v7\n"
      "v_add_u32 v156, 98304, v7\n"
      "v_xor_b32 v7, 16, v7\n"
      "v_add_u32 v157, 98560, v7\n"
      "s_bfe_u32 s91, s18, 0x10000\n"
      "s_lshl_b32 s91, s91, 22\n"
      "s_bfe_u32 s85, s18, 0x40004\n"
      "s_lshl_b32 s85, s85, 18\n"
      "s_add_u32 s91, s91, s85\n"
      "s_bfe_u32 s85, s18, 0x20001\n"
      "s_lshl_b32 s85, s85, 16\n"
      "buffer_load_dwordx4 v159, s[28:31], s73 offen offset:1024 nt lds\n"
      "s_add_u32 s91, s91, s85\n"
      "s_lshl_b32 s85, s19, 14\n"
      "s_add_u32 s91, s91, s85\n"
      "s_add_u32 s91, s91, 1048576\n"
      "s_bfe_u32 s56, s18, 0x30000\n"
      "s_lshl_b32 s56, s56, 20\n"
      "s_bfe_u32 s85, s18, 0x40004\n"
      "s_lshl_b32 s85, s85, 16\n"
      "s_add_u32 s56, s56, s85\n"
      "s_lshl_b32 s85, s19, 14\n"
      "s_add_u32 s56, s56, s85\n"
      "s_add_u32 s56, s56, 9437184\n"
      "s_add_u32 s57, s56, 8192\n"
      "s_bfe_u32 s59, s18, 0x20001\n"
      "s_lshl_b32 s59, s59, 20\n"
      "s_bfe_u32 s85, s18, 0x10000\n"
      "s_lshl_b32 s85, s85, 19\n"
      "s_add_u32 s59, s59, s85\n"
      "s_bfe_u32 s85, s18, 0x40004\n"
      "s_lshl_b32 s85, s85, 15\n"
      "buffer_load_dwordx4 v160, s[28:31], s73 offen offset:2048 nt lds\n"
      "s_add_u32 s59, s59, s85\n"
      "s_lshl_b32 s85, s19, 13\n"
      "s_add_u32 s59, s59, s85\n"
      "s_add_u32 s59, s59, 17825792\n"
      "s_bfe_u32 s60, s18, 0x30000\n"
      "s_lshl_b32 s60, s60, 11\n"
      "s_bfe_u32 s85, s18, 0x20006\n"
      "s_lshl_b32 s85, s85, 9\n"
      "s_add_u32 s60, s60, s85\n"
      "s_bfe_u32 s85, s18, 0x20004\n"
      "s_lshl_b32 s85, s85, 7\n"
      "s_add_u32 s60, s60, s85\n"
      "s_add_u32 s60, s60, 8192\n"
      "s_bfe_u32 s61, s18, 0x20001\n"
      "s_lshl_b32 s61, s61, 12\n"
      "s_bfe_u32 s85, s18, 0x30004\n"
      "s_lshl_b32 s85, s85, 9\n"
      "s_add_u32 s61, s61, s85\n"
      "s_add_u32 s61, s61, 8192\n"
      "buffer_load_dwordx4 v161, s[28:31], s73 offen offset:3072 nt lds\n"
      "s_bfe_u32 s62, s18, 0x20001\n"
      "s_lshl_b32 s62, s62, 20\n"
      "s_bfe_u32 s85, s18, 0x30004\n"
      "s_lshl_b32 s85, s85, 17\n"
      "s_add_u32 s62, s62, s85\n"
      "s_lshl_b32 s85, s19, 12\n"
      "s_add_u32 s62, s62, s85\n"
      "s_add_u32 s62, s62, 17825792\n"
      "s_bfe_u32 s63, s18, 0x10000\n"
      "s_lshl_b32 s63, s63, 22\n"
      "s_bfe_u32 s85, s18, 0x50003\n"
      "s_lshl_b32 s85, s85, 17\n"
      "s_add_u32 s63, s63, s85\n"
      "s_lshl_b32 s85, s19, 11\n"
      "s_add_u32 s63, s63, s85\n"
      "s_add_u32 s63, s63, 22020096\n"
      "s_bfe_u32 s64, s18, 0x10000\n"
      "s_lshl_b32 s64, s64, 14\n"
      "s_bfe_u32 s85, s18, 0x50003\n"
      "s_lshl_b32 s85, s85, 9\n"
      "s_add_u32 m0, s72, 16384\n"
      "s_nop 0\n"
      "buffer_load_dwordx4 v162, s[36:39], s74 offen offset:0  lds\n"
      "s_add_u32 s64, s64, s85\n"
      "s_add_u32 s64, s64, 24576\n"
      "s_bfe_u32 s65, s18, 0x30004\n"
      "s_lshl_b32 s65, s65, 17\n"
      "s_bfe_u32 s85, s18, 0x20001\n"
      "s_lshl_b32 s85, s85, 15\n"
      "s_add_u32 s65, s65, s85\n"
      "s_mov_b32 s85, s17\n"
      "s_lshl_b32 s85, s85, 8\n"
      "s_add_u32 s65, s65, s85\n"
      "s_lshl_b32 s85, s19, 6\n"
      "s_add_u32 s65, s65, s85\n"
      "v_mov_b32 v64, 0\n"
      "v_mov_b32 v65, 0\n"
      "v_mov_b32 v66, 0\n"
      "v_mov_b32 v67, 0\n"
      "v_mov_b32 v68, 0\n"
      "v_mov_b32 v69, 0\n"
      "v_mov_b32 v70, 0\n"
      "buffer_load_dwordx4 v163, s[36:39], s74 offen offset:1024  lds\n"
      "v_mov_b32 v71, 0\n"
      "v_mov_b32 v72, 0\n"
      "v_mov_b32 v73, 0\n"
      "v_mov_b32 v74, 0\n"
      "v_mov_b32 v75, 0\n"
      "v_mov_b32 v76, 0\n"
      "v_mov_b32 v77, 0\n"
      "v_mov_b32 v78, 0\n"
      "v_mov_b32 v79, 0\n"
      "v_mov_b32 v80, 0\n"
      "v_mov_b32 v81, 0\n"
      "v_mov_b32 v82, 0\n"
      "v_mov_b32 v83, 0\n"
      "v_mov_b32 v84, 0\n"
      "v_mov_b32 v85, 0\n"
      "v_mov_b32 v86, 0\n"
      "v_mov_b32 v87, 0\n"
      "v_mov_b32 v88, 0\n"
      "v_mov_b32 v89, 0\n"
      "v_mov_b32 v90, 0\n"
      "buffer_load_dwordx4 v164, s[36:39], s74 offen offset:2048  lds\n"
      "v_mov_b32 v91, 0\n"
      "v_mov_b32 v92, 0\n"
      "v_mov_b32 v93, 0\n"
      "v_mov_b32 v94, 0\n"
      "v_mov_b32 v95, 0\n"
      "v_mov_b32 v96, 0\n"
      "v_mov_b32 v97, 0\n"
      "v_mov_b32 v98, 0\n"
      "v_mov_b32 v99, 0\n"
      "v_mov_b32 v100, 0\n"
      "v_mov_b32 v101, 0\n"
      "v_mov_b32 v102, 0\n"
      "v_mov_b32 v103, 0\n"
      "v_mov_b32 v104, 0\n"
      "v_mov_b32 v105, 0\n"
      "v_mov_b32 v106, 0\n"
      "v_mov_b32 v107, 0\n"
      "v_mov_b32 v108, 0\n"
      "v_mov_b32 v109, 0\n"
      "buffer_load_dwordx4 v165, s[36:39], s74 offen offset:3072  lds\n"
      "v_mov_b32 v110, 0\n"
      "v_mov_b32 v111, 0\n"
      "v_mov_b32 v112, 0\n"
      "v_mov_b32 v113, 0\n"
      "v_mov_b32 v114, 0\n"
      "v_mov_b32 v115, 0\n"
      "v_mov_b32 v116, 0\n"
      "v_mov_b32 v117, 0\n"
      "v_mov_b32 v118, 0\n"
      "v_mov_b32 v119, 0\n"
      "v_mov_b32 v120, 0\n"
      "v_mov_b32 v121, 0\n"
      "v_mov_b32 v122, 0\n"
      "v_mov_b32 v123, 0\n"
      "v_mov_b32 v124, 0\n"
      "v_mov_b32 v125, 0\n"
      "v_mov_b32 v126, 0\n"
      "v_mov_b32 v127, 0\n"
      "v_mov_b32 v128, 0\n"
      "v_mov_b32 v129, 0\n"
      "s_mov_b32 s86, 0\n"
      "s_cmp_lg_u32 s86, 0\n"
      "s_cbranch_scc1 .Lgq_pf5_a\n"
      "s_getpc_b64 s[50:51]\n"
      "s_branch .Lgq_qload\n"
      ".Lgq_pf5_a:\n"
      "s_add_u32 m0, s72, 32768\n"
      "v_mfma_f32_32x32x16_bf16 v[16:31], a[48:51], a[64:67], v[16:31]\n"
      "v_mfma_f32_32x32x16_bf16 v[32:47], a[52:55], a[68:71], v[32:47]\n"
      "v_mfma_f32_32x32x16_bf16 v[16:31], a[56:59], a[72:75], v[16:31]\n"
      "v_mfma_f32_32x32x16_bf16 v[32:47], a[32:35], a[76:79], v[32:47]\n"
      "buffer_load_dwordx4 v158, s[32:35], s73 offen offset:0 nt lds\n"
      "v_mfma_f32_32x32x16_bf16 v[16:31], a[36:39], a[64:67], v[16:31]\n"
      "v_mfma_f32_32x32x16_bf16 v[32:47], a[40:43], a[68:71], v[32:47]\n"
      "v_mfma_f32_32x32x16_bf16 v[16:31], a[44:47], a[72:75], v[16:31]\n"
      "v_mfma_f32_32x32x16_bf16 v[32:47], a[48:51], a[76:79], v[32:47]\n"
      "buffer_load_dwordx4 v159, s[32:35], s73 offen offset:1024 nt lds\n"
      "v_mfma_f32_32x32x16_bf16 v[16:31], a[52:55], a[64:67], v[16:31]\n"
      "v_mfma_f32_32x32x16_bf16 v[32:47], a[56:59], a[68:71], v[32:47]\n"
      "v_mfma_f32_32x32x16_bf16 v[16:31], a[32:35], a[72:75], v[16:31]\n"
      "v_mfma_f32_32x32x16_bf16 v[32:47], a[36:39], a[76:79], v[32:47]\n"
      "buffer_load_dwordx4 v160, s[32:35], s73 offen offset:2048 nt lds\n"
      "v_mfma_f32_32x32x16_bf16 v[16:31], a[40:43], a[64:67], v[16:31]\n"
      "v_mfma_f32_32x32x16_bf16 v[32:47], a[44:47], a[68:71], v[32:47]\n"
      "v_mfma_f32_32x32x16_bf16 v[16:31], a[48:51], a[72:75], v[16:31]\n"
      "v_mfma_f32_32x32x16_bf16 v[32:47], a[52:55], a[76:79], v[32:47]\n"
      "buffer_load_dwordx4 v161, s[32:35], s73 offen offset:3072 nt lds\n"
      "s_add_u32 s73, s73, 16384\n"
      "s_add_u32 s74, s74, 0x20000\n"
      "s_and_b32 s74, s74, 0x1fffff\n"
      "s_add_u32 s72, s72, 49152\n"
      "s_cmp_ge_u32 s72, 147456\n"
      "s_cselect_b32 s99, 147456, 0\n"
      "s_sub_u32 s72, s72, s99\n"
      "s_cmp_lg_u32 s86, 0\n"
      "s_cbranch_scc1 .Lgq_pf5_b\n"
      "s_waitcnt vmcnt(4)\n"
      "s_getpc_b64 s[50:51]\n"
      "s_branch .Lgq_qchk\n"
      ".Lgq_pf5_b:\n"
      "s_mov_b32 s87, 65536\n"
      ".Lgq_q_spin:\n"
      "s_cmp_lg_u32 s86, 0\n"
      "s_cbranch_scc1 .Lgq_q_have\n"
      "s_getpc_b64 s[50:51]\n"
      "s_branch .Lgq_qload\n"
      "s_waitcnt vmcnt(0)\n"
      "s_getpc_b64 s[50:51]\n"
      "s_branch .Lgq_qchk\n"
      "s_sub_u32 s87, s87, 1\n"
      "s_cmp_lg_u32 s87, 0\n"
      "s_cbranch_scc1 .Lgq_q_spin\n"
      ".Lgq_q_have:\n"
      "v_mov_b32 v179, v48\n"
      "v_mov_b32 v183, v49\n"
      "v_mov_b32 v187, v52\n"
      "v_mov_b32 v191, v53\n"
      "v_mov_b32 v195, v56\n"
      "v_mov_b32 v199, v57\n"
      "v_mov_b32 v203, v60\n"
      "v_mov_b32 v207, v61\n"
      "s_barrier\n"
      "ds_read_b128 a[32:35], v152 offset:0\n"
      "ds_read_b128 a[36:39], v153 offset:0\n"
      "ds_read_b128 a[40:43], v152 offset:512\n"
      "ds_read_b128 a[44:47], v153 offset:512\n"
      "ds_read_b128 a[48:51], v152 offset:1024\n"
      "ds_read_b128 a[52:55], v153 offset:1024\n"
      "ds_read_b128 a[56:59], v152 offset:1536\n"
      "ds_read_b128 a[60:63], v153 offset:1536\n"
      "s_getpc_b64 s[52:53]\n"
      ".Lgq_wpc:\n"
      "s_add_u32 s52, s52, .Lgq_wtab-.Lgq_wpc\n"
      "s_addc_u32 s53, s53, 0\n"
      "s_mov_b32 s75, 1\n"
      "s_waitcnt lgkmcnt(0)\n"
      "v_mfma_f32_32x32x16_bf16 v[16:31], a[32:35], v[176:179], 0\n"
      "ds_read_b64_tr_b16 a[112:113], v156 offset:16384\n"
      "v_mfma_f32_32x32x16_bf16 v[16:31], a[36:39], v[180:183], v[16:31]\n"
      "ds_read_b64_tr_b16 a[114:115], v157 offset:16384\n"
      "v_mfma_f32_32x32x16_bf16 v[16:31], a[40:43], v[184:187], v[16:31]\n"
      "ds_read_b64_tr_b16 a[116:117], v156 offset:16392\n"
      "v_mfma_f32_32x32x16_bf16 v[16:31], a[44:47], v[188:191], v[16:31]\n"
      "ds_read_b64_tr_b16 a[118:119], v157 offset:16392\n"
      "v_mfma_f32_32x32x16_bf16 v[16:31], a[48:51], v[192:195], v[16:31]\n"
      "ds_read_b64_tr_b16 a[120:121], v156 offset:24576\n"
      "v_mfma_f32_32x32x16_bf16 v[16:31], a[52:55], v[196:199], v[16:31]\n"
      "ds_read_b64_tr_b16 a[122:123], v157 offset:24576\n"
      "v_mfma_f32_32x32x16_bf16 v[16:31], a[56:59], v[200:203], v[16:31]\n"
      "ds_read_b64_tr_b16 a[124:125], v156 offset:24584\n"
      "v_mfma_f32_32x32x16_bf16 v[16:31], a[60:63], v[204:207], v[16:31]\n"
      "ds_read_b64_tr_b16 a[126:127], v157 offset:24584\n"
      "ds_read_b128 a[32:35], v152 offset:8192\n"
      "ds_read_b128 a[36:39], v153 offset:8192\n"
      "ds_read_b128 a[40:43], v152 offset:8704\n"
      "ds_read_b128 a[44:47], v153 offset:8704\n"
      "ds_read_b128 a[48:51], v152 offset:9216\n"
      "ds_read_b128 a[52:55], v153 offset:9216\n"
      "ds_read_b128 a[56:59], v152 offset:9728\n"
      "ds_read_b128 a[60:63], v153 offset:9728\n"
      "s_waitcnt lgkmcnt(8)\n"
      "ds_read_b64_tr_b16 a[64:65], v154 offset:32768\n"
      "ds_read_b64_tr_b16 a[66:67], v155 offset:32768\n"
      "ds_read_b64_tr_b16 a[68:69], v154 offset:33280\n"
      "ds_read_b64_tr_b16 a[70:71], v155 offset:33280\n"
      "ds_read_b64_tr_b16 a[72:73], v154 offset:33792\n"
      "ds_read_b64_tr_b16 a[74:75], v155 offset:33792\n"
      "ds_read_b64_tr_b16 a[76:77], v154 offset:34304\n"
      "ds_read_b64_tr_b16 a[78:79], v155 offset:34304\n"
      "v_max3_f32 v173, v16, v17, v18\n"
      "v_max3_f32 v173, v173, v19, v20\n"
      "v_max3_f32 v173, v173, v21, v22\n"
      "v_max3_f32 v173, v173, v23, v24\n"
      "v_max3_f32 v173, v173, v25, v26\n"
      "v_max3_f32 v173, v173, v27, v28\n"
      "v_max3_f32 v173, v173, v29, v30\n"
      "v_max_f32 v173, v173, v31\n"
      "v_mov_b32 v6, v173\n"
      "v_mov_b32 v7, v173\n"
      "s_nop 1\n"
      "v_permlane32_swap_b32 v6, v7\n"
      "v_max_f32 v173, v6, v7\n"
      "v_sub_f32 v16, v16, v173\n"
      "v_sub_f32 v17, v17, v173\n"
      "v_sub_f32 v18, v18, v173\n"
      "v_sub_f32 v19, v19, v173\n"
      "v_sub_f32 v20, v20, v173\n"
      "v_sub_f32 v21, v21, v173\n"
      "v_sub_f32 v22, v22, v173\n"
      "v_sub_f32 v23, v23, v173\n"
      "v_sub_f32 v24, v24, v173\n"
      "v_sub_f32 v25, v25, v173\n"
      "v_sub_f32 v26, v26, v173\n"
      "v_sub_f32 v27, v27, v173\n"
      "v_sub_f32 v28, v28, v173\n"
      "v_sub_f32 v29, v29, v173\n"
      "v_sub_f32 v30, v30, v173\n"
      "v_sub_f32 v31, v31, v173\n"
      "v_sub_f32 v48, 0, v173\n"
      "v_sub_f32 v49, 0, v173\n"
      "v_sub_f32 v50, 0, v173\n"
      "v_sub_f32 v51, 0, v173\n"
      "v_sub_f32 v52, 0, v173\n"
      "v_sub_f32 v53, 0, v173\n"
      "v_sub_f32 v54, 0, v173\n"
      "v_sub_f32 v55, 0, v173\n"
      "v_sub_f32 v56, 0, v173\n"
      "v_sub_f32 v57, 0, v173\n"
      "v_sub_f32 v58, 0, v173\n"
      "v_sub_f32 v59, 0, v173\n"
      "v_sub_f32 v60, 0, v173\n"
      "v_sub_f32 v61, 0, v173\n"
      "v_sub_f32 v62, 0, v173\n"
      "v_sub_f32 v63, 0, v173\n"
      "s_mov_b32 s75, 0\n"
      ".Lgq_bs_loop:\n"
      "s_waitcnt lgkmcnt(8)\n"
      "v_mfma_f32_32x32x16_bf16 v[32:47], a[32:35], v[176:179], v[48:63]\n"
      "ds_read_b64_tr_b16 a[80:81], v154 offset:36864\n"
      "v_exp_f32 v16, v16\n"
      "v_exp_f32 v17, v17\n"
      "v_mfma_f32_32x32x16_bf16 v[32:47], a[36:39], v[180:183], v[32:47]\n"
      "ds_read_b64_tr_b16 a[82:83], v155 offset:36864\n"
      "v_exp_f32 v18, v18\n"
      "v_exp_f32 v19, v19\n"
      "v_cvt_pk_bf16_f32 v144, v16, v17\n"
      "v_mfma_f32_32x32x16_bf16 v[32:47], a[40:43], v[184:187], v[32:47]\n"
      "ds_read_b64_tr_b16 a[84:85], v154 offset:37376\n"
      "v_exp_f32 v20, v20\n"
      "v_exp_f32 v21, v21\n"
      "v_cvt_pk_bf16_f32 v145, v18, v19\n"
      "v_mfma_f32_32x32x16_bf16 v[32:47], a[44:47], v[188:191], v[32:47]\n"
      "ds_read_b64_tr_b16 a[86:87], v155 offset:37376\n"
      "v_exp_f32 v22, v22\n"
      "v_exp_f32 v23, v23\n"
      "v_cvt_pk_bf16_f32 v146, v20, v21\n"
      "v_mfma_f32_32x32x16_bf16 v[32:47], a[48:51], v[192:195], v[32:47]\n"
      "ds_read_b64_tr_b16 a[88:89], v154 offset:37888\n"
      "v_exp_f32 v24, v24\n"
      "v_exp_f32 v25, v25\n"
      "v_cvt_pk_bf16_f32 v147, v22, v23\n"
      "v_mfma_f32_32x32x16_bf16 v[32:47], a[52:55], v[196:199], v[32:47]\n"
      "ds_read_b64_tr_b16 a[90:91], v155 offset:37888\n"
      "v_exp_f32 v26, v26\n"
      "v_exp_f32 v27, v27\n"
      "v_cvt_pk_bf16_f32 v148, v24, v25\n"
      "v_mfma_f32_32x32x16_bf16 v[32:47], a[56:59], v[200:203], v[32:47]\n"
      "ds_read_b64_tr_b16 a[92:93], v154 offset:38400\n"
      "v_exp_f32 v28, v28\n"
      "v_exp_f32 v29, v29\n"
      "v_cvt_pk_bf16_f32 v149, v26, v27\n"
      "v_mfma_f32_32x32x16_bf16 v[32:47], a[60:63], v[204:207], v[32:47]\n"
      "ds_read_b64_tr_b16 a[94:95], v155 offset:38400\n"
      "v_exp_f32 v30, v30\n"
      "v_exp_f32 v31, v31\n"
      "v_cvt_pk_bf16_f32 v150, v28, v29\n"
      "v_cvt_pk_bf16_f32 v151, v30, v31\n"
      "s_waitcnt lgkmcnt(8)\n"
      "v_mfma_f32_32x32x16_bf16 v[64:79], a[64:67], v[144:147], v[64:79]\n"
      "s_cmp_eq_u32 s77, 98304\n"
      "s_cselect_b32 s76, 4294868992, s78\n"
      "s_add_u32 s77, s77, s76\n"
      "v_add_u32 v152, s76, v152\n"
      "v_add_u32 v153, s76, v153\n"
      "v_add_f32 v128, v128, v16\n"
      "v_mfma_f32_32x32x16_bf16 v[80:95], a[68:71], v[144:147], v[80:95]\n"
      "s_waitcnt vmcnt(8) lgkmcnt(0)\n"
      "s_barrier\n"
      "ds_read_b128 a[32:35], v152 offset:0\n"
      "ds_read_b128 a[36:39], v153 offset:0\n"
      "s_mov_b32 m0, s72\n"
      "v_add_f32 v129, v129, v17\n"
      "v_add_f32 v128, v128, v18\n"
      "v_mfma_f32_32x32x16_bf16 v[96:111], a[72:75], v[144:147], v[96:111]\n"
      "ds_read_b128 a[40:43], v152 offset:512\n"
      "ds_read_b128 a[44:47], v153 offset:512\n"
      "buffer_load_dwordx4 v158, s[28:31], s73 offen offset:0 nt lds\n"
      "v_add_f32 v129, v129, v19\n"
      "v_mfma_f32_32x32x16_bf16 v[112:127], a[76:79], v[144:147], v[112:127]\n"
      "ds_read_b128 a[48:51], v152 offset:1024\n"
      "ds_read_b128 a[52:55], v153 offset:1024\n"
      "v_max3_f32 v173, v32, v33, v34\n"
      "v_max3_f32 v173, v173, v35, v36\n"
      "v_add_f32 v128, v128, v20\n"
      "v_add_f32 v129, v129, v21\n"
      "ds_read_b128 a[56:59], v152 offset:1536\n"
      "ds_read_b128 a[60:63], v153 offset:1536\n"
      "buffer_load_dwordx4 v159, s[28:31], s73 offen offset:1024 nt lds\n"
      "v_add_f32 v128, v128, v22\n"
      "v_add_f32 v129, v129, v23\n"
      "v_mfma_f32_32x32x16_bf16 v[64:79], a[80:83], v[148:151], v[64:79]\n"
      "ds_read_b64_tr_b16 a[64:65], v154 offset:40960\n"
      "ds_read_b64_tr_b16 a[66:67], v155 offset:40960\n"
      "v_max3_f32 v173, v173, v37, v38\n"
      "v_max3_f32 v173, v173, v39, v40\n"
      "v_max3_f32 v173, v173, v41, v42\n"
      "v_add_f32 v128, v128, v24\n"
      "v_mfma_f32_32x32x16_bf16 v[80:95], a[84:87], v[148:151], v[80:95]\n"
      "ds_read_b64_tr_b16 a[68:69], v154 offset:41472\n"
      "ds_read_b64_tr_b16 a[70:71], v155 offset:41472\n"
      "buffer_load_dwordx4 v160, s[28:31], s73 offen offset:2048 nt lds\n"
      "v_add_f32 v129, v129, v25\n"
      "v_add_f32 v128, v128, v26\n"
      "v_mfma_f32_32x32x16_bf16 v[96:111], a[88:91], v[148:151], v[96:111]\n"
      "ds_read_b64_tr_b16 a[72:73], v154 offset:41984\n"
      "ds_read_b64_tr_b16 a[74:75], v155 offset:41984\n"
      "v_max3_f32 v173, v173, v43, v44\n"
      "v_max3_f32 v173, v173, v45, v46\n"
      "v_max_f32 v173, v173, v47\n"
      "v_add_f32 v129, v129, v27\n"
      "v_mfma_f32_32x32x16_bf16 v[112:127], a[92:95], v[148:151], v[112:127]\n"
      "ds_read_b64_tr_b16 a[76:77], v154 offset:42496\n"
      "ds_read_b64_tr_b16 a[78:79], v155 offset:42496\n"
      "buffer_load_dwordx4 v161, s[28:31], s73 offen offset:3072 nt lds\n"
      "v_cmp_lt_f32 vcc, s92, v173\n"
      "v_add_f32 v128, v128, v28\n"
      "v_add_f32 v129, v129, v29\n"
      "s_add_u32 m0, s72, 16384\n"
      "v_add_f32 v128, v128, v30\n"
      "v_add_f32 v129, v129, v31\n"
      "s_cbranch_vccnz .Lgq_bs0_rare\n"
      ".Lgq_bs0_ret:\n"
      "s_waitcnt lgkmcnt(8)\n"
      "v_mfma_f32_32x32x16_bf16 v[16:31], a[32:35], v[176:179], v[48:63]\n"
      "ds_read_b64_tr_b16 a[80:81], v154 offset:45056\n"
      "v_exp_f32 v32, v32\n"
      "v_exp_f32 v33, v33\n"
      "v_mfma_f32_32x32x16_bf16 v[16:31], a[36:39], v[180:183], v[16:31]\n"
      "ds_read_b64_tr_b16 a[82:83], v155 offset:45056\n"
      "buffer_load_dwordx4 v162, s[36:39], s74 offen offset:0  lds\n"
      "v_exp_f32 v34, v34\n"
      "v_exp_f32 v35, v35\n"
      "v_cvt_pk_bf16_f32 v144, v32, v33\n"
      "v_mfma_f32_32x32x16_bf16 v[16:31], a[40:43], v[184:187], v[16:31]\n"
      "ds_read_b64_tr_b16 a[84:85], v154 offset:45568\n"
      "v_exp_f32 v36, v36\n"
      "v_exp_f32 v37, v37\n"
      "v_cvt_pk_bf16_f32 v145, v34, v35\n"
      "v_mfma_f32_32x32x16_bf16 v[16:31], a[44:47], v[188:191], v[16:31]\n"
      "ds_read_b64_tr_b16 a[86:87], v155 offset:45568\n"
      "buffer_load_dwordx4 v163, s[36:39], s74 offen offset:1024  lds\n"
      "v_exp_f32 v38, v38\n"
      "v_exp_f32 v39, v39\n"
      "v_cvt_pk_bf16_f32 v146, v36, v37\n"
      "v_mfma_f32_32x32x16_bf16 v[16:31], a[48:51], v[192:195], v[16:31]\n"
      "ds_read_b64_tr_b16 a[88:89], v154 offset:46080\n"
      "v_exp_f32 v40, v40\n"
      "v_exp_f32 v41, v41\n"
      "v_cvt_pk_bf16_f32 v147, v38, v39\n"
      "v_mfma_f32_32x32x16_bf16 v[16:31], a[52:55], v[196:199], v[16:31]\n"
      "ds_read_b64_tr_b16 a[90:91], v155 offset:46080\n"
      "buffer_load_dwordx4 v164, s[36:39], s74 offen offset:2048  lds\n"
      "v_exp_f32 v42, v42\n"
      "v_exp_f32 v43, v43\n"
      "v_cvt_pk_bf16_f32 v148, v40, v41\n"
      "v_mfma_f32_32x32x16_bf16 v[16:31], a[56:59], v[200:203], v[16:31]\n"
      "ds_read_b64_tr_b16 a[92:93], v154 offset:46592\n"
      "v_exp_f32 v44, v44\n"
      "v_exp_f32 v45, v45\n"
      "v_cvt_pk_bf16_f32 v149, v42, v43\n"
      "v_mfma_f32_32x32x16_bf16 v[16:31], a[60:63], v[204:207], v[16:31]\n"
      "ds_read_b64_tr_b16 a[94:95], v155 offset:46592\n"
      "buffer_load_dwordx4 v165, s[36:39], s74 offen offset:3072  lds\n"
      "v_exp_f32 v46, v46\n"
      "v_exp_f32 v47, v47\n"
      "v_cvt_pk_bf16_f32 v150, v44, v45\n"
      "v_cvt_pk_bf16_f32 v151, v46, v47\n"
      "s_waitcnt lgkmcnt(8)\n"
      "v_mfma_f32_32x32x16_bf16 v[64:79], a[64:67], v[144:147], v[64:79]\n"
      "v_add_u32 v154, s76, v154\n"
      "v_add_u32 v155, s76, v155\n"
      "v_add_u32 v156, s76, v156\n"
      "v_add_u32 v157, s76, v157\n"
      "v_add_f32 v128, v128, v32\n"
      "v_mfma_f32_32x32x16_bf16 v[80:95], a[68:71], v[144:147], v[80:95]\n"
      "s_waitcnt vmcnt(8) lgkmcnt(0)\n"
      "s_barrier\n"
      "ds_read_b128 a[32:35], v152 offset:8192\n"
      "ds_read_b128 a[36:39], v153 offset:8192\n"
      "s_add_u32 m0, s72, 32768\n"
      "v_add_f32 v129, v129, v33\n"
      "v_add_f32 v128, v128, v34\n"
      "v_mfma_f32_32x32x16_bf16 v[96:111], a[72:75], v[144:147], v[96:111]\n"
      "ds_read_b128 a[40:43], v152 offset:8704\n"
      "ds_read_b128 a[44:47], v153 offset:8704\n"
      "buffer_load_dwordx4 v158, s[32:35], s73 offen offset:0 nt lds\n"
      "v_add_f32 v129, v129, v35\n"
      "v_mfma_f32_32x32x16_bf16 v[112:127], a[76:79], v[144:147], v[112:127]\n"
      "ds_read_b128 a[48:51], v152 offset:9216\n"
      "ds_read_b128 a[52:55], v153 offset:9216\n"
      "v_max3_f32 v173, v16, v17, v18\n"
      "v_max3_f32 v173, v173, v19, v20\n"
      "v_add_f32 v128, v128, v36\n"
      "v_add_f32 v129, v129, v37\n"
      "ds_read_b128 a[56:59], v152 offset:9728\n"
      "ds_read_b128 a[60:63], v153 offset:9728\n"
      "buffer_load_dwordx4 v159, s[32:35], s73 offen offset:1024 nt lds\n"
      "v_add_f32 v128, v128, v38\n"
      "v_add_f32 v129, v129, v39\n"
      "v_mfma_f32_32x32x16_bf16 v[64:79], a[80:83], v[148:151], v[64:79]\n"
      "s_swappc_b64 s[50:51], s[52:53]\n"
      "v_max3_f32 v173, v173, v21, v22\n"
      "v_max3_f32 v173, v173, v23, v24\n"
      "v_max3_f32 v173, v173, v25, v26\n"
      "v_add_f32 v128, v128, v40\n"
      "v_mfma_f32_32x32x16_bf16 v[80:95], a[84:87], v[148:151], v[80:95]\n"
      "s_waitcnt lgkmcnt(8)\n"
      "ds_read_b64_tr_b16 a[64:65], v154 offset:32768\n"
      "ds_read_b64_tr_b16 a[66:67], v155 offset:32768\n"
      "buffer_load_dwordx4 v160, s[32:35], s73 offen offset:2048 nt lds\n"
      "v_add_f32 v129, v129, v41\n"
      "v_add_f32 v128, v128, v42\n"
      "v_mfma_f32_32x32x16_bf16 v[96:111], a[88:91], v[148:151], v[96:111]\n"
      "ds_read_b64_tr_b16 a[68:69], v154 offset:33280\n"
      "ds_read_b64_tr_b16 a[70:71], v155 offset:33280\n"
      "v_max3_f32 v173, v173, v27, v28\n"
      "v_max3_f32 v173, v173, v29, v30\n"
      "v_max_f32 v173, v173, v31\n"
      "v_add_f32 v129, v129, v43\n"
      "v_mfma_f32_32x32x16_bf16 v[112:127], a[92:95], v[148:151], v[112:127]\n"
      "ds_read_b64_tr_b16 a[72:73], v154 offset:33792\n"
      "ds_read_b64_tr_b16 a[74:75], v155 offset:33792\n"
      "buffer_load_dwordx4 v161, s[32:35], s73 offen offset:3072 nt lds\n"
      "v_cmp_lt_f32 vcc, s92, v173\n"
      "v_add_f32 v128, v128, v44\n"
      "v_add_f32 v129, v129, v45\n"
      "ds_read_b64_tr_b16 a[76:77], v154 offset:34304\n"
      "ds_read_b64_tr_b16 a[78:79], v155 offset:34304\n"
      "s_add_u32 s73, s73, 16384\n"
      "s_add_u32 s74, s74, 0x20000\n"
      "s_and_b32 s74, s74, 0x1fffff\n"
      "s_add_u32 s72, s72, 49152\n"
      "s_cmp_ge_u32 s72, 147456\n"
      "s_cselect_b32 s99, 147456, 0\n"
      "s_sub_u32 s72, s72, s99\n"
      "s_add_u32 s52, s52, 72\n"
      "s_addc_u32 s53, s53, 0\n"
      "s_add_u32 s75, s75, 1\n"
      "v_add_f32 v128, v128, v46\n"
      "v_add_f32 v129, v129, v47\n"
      "s_cbranch_vccnz .Lgq_bs1_rare\n"
      ".Lgq_bs1_ret:\n"
      "s_cmp_lg_u32 s75, s98\n"
      "s_cbranch_scc1 .Lgq_bs_loop\n"
      "s_cmp_lg_u32 s79, 0\n"
      "s_cbranch_scc1 .Lgq_bk_skip\n"
      ".Lgq_bk_loop:\n"
      "s_waitcnt lgkmcnt(8)\n"
      "v_mfma_f32_32x32x16_bf16 v[32:47], a[32:35], v[176:179], v[48:63]\n"
      "ds_read_b64_tr_b16 a[80:81], v154 offset:36864\n"
      "v_exp_f32 v16, v16\n"
      "v_exp_f32 v17, v17\n"
      "v_mfma_f32_32x32x16_bf16 v[32:47], a[36:39], v[180:183], v[32:47]\n"
      "ds_read_b64_tr_b16 a[82:83], v155 offset:36864\n"
      "v_exp_f32 v18, v18\n"
      "v_exp_f32 v19, v19\n"
      "v_cvt_pk_bf16_f32 v144, v16, v17\n"
      "v_mfma_f32_32x32x16_bf16 v[32:47], a[40:43], v[184:187], v[32:47]\n"
      "ds_read_b64_tr_b16 a[84:85], v154 offset:37376\n"
      "v_exp_f32 v20, v20\n"
      "v_exp_f32 v21, v21\n"
      "v_cvt_pk_bf16_f32 v145, v18, v19\n"
      "v_mfma_f32_32x32x16_bf16 v[32:47], a[44:47], v[188:191], v[32:47]\n"
      "ds_read_b64_tr_b16 a[86:87], v155 offset:37376\n"
      "v_exp_f32 v22, v22\n"
      "v_exp_f32 v23, v23\n"
      "v_cvt_pk_bf16_f32 v146, v20, v21\n"
      "v_mfma_f32_32x32x16_bf16 v[32:47], a[48:51], v[192:195], v[32:47]\n"
      "ds_read_b64_tr_b16 a[88:89], v154 offset:37888\n"
      "v_exp_f32 v24, v24\n"
      "v_exp_f32 v25, v25\n"
      "v_cvt_pk_bf16_f32 v147, v22, v23\n"
      "v_mfma_f32_32x32x16_bf16 v[32:47], a[52:55], v[196:199], v[32:47]\n"
      "ds_read_b64_tr_b16 a[90:91], v155 offset:37888\n"
      "v_exp_f32 v26, v26\n"
      "v_exp_f32 v27, v27\n"
      "v_cvt_pk_bf16_f32 v148, v24, v25\n"
      "v_mfma_f32_32x32x16_bf16 v[32:47], a[56:59], v[200:203], v[32:47]\n"
      "ds_read_b64_tr_b16 a[92:93], v154 offset:38400\n"
      "v_exp_f32 v28, v28\n"
      "v_exp_f32 v29, v29\n"
      "v_cvt_pk_bf16_f32 v149, v26, v27\n"
      "v_mfma_f32_32x32x16_bf16 v[32:47], a[60:63], v[204:207], v[32:47]\n"
      "ds_read_b64_tr_b16 a[94:95], v155 offset:38400\n"
      "v_exp_f32 v30, v30\n"
      "v_exp_f32 v31, v31\n"
      "v_cvt_pk_bf16_f32 v150, v28, v29\n"
      "v_cvt_pk_bf16_f32 v151, v30, v31\n"
      "s_waitcnt lgkmcnt(8)\n"
      "v_mfma_f32_32x32x16_bf16 v[64:79], a[64:67], v[144:147], v[64:79]\n"
      "s_cmp_eq_u32 s77, 98304\n"
      "s_cselect_b32 s76, 4294868992, s78\n"
      "s_add_u32 s77, s77, s76\n"
      "v_add_u32 v152, s76, v152\n"
      "v_add_u32 v153, s76, v153\n"
      "v_add_f32 v128, v128, v16\n"
      "v_mfma_f32_32x32x16_bf16 v[80:95], a[68:71], v[144:147], v[80:95]\n"
      "s_waitcnt vmcnt(4) lgkmcnt(0)\n"
      "s_barrier\n"
      "ds_read_b128 a[32:35], v152 offset:0\n"
      "ds_read_b128 a[36:39], v153 offset:0\n"
      "s_mov_b32 m0, s72\n"
      "v_add_f32 v129, v129, v17\n"
      "v_add_f32 v128, v128, v18\n"
      "v_mfma_f32_32x32x16_bf16 v[96:111], a[72:75], v[144:147], v[96:111]\n"
      "ds_read_b128 a[40:43], v152 offset:512\n"
      "ds_read_b128 a[44:47], v153 offset:512\n"
      "buffer_load_dwordx4 v158, s[28:31], s73 offen offset:0 nt lds\n"
      "v_add_f32 v129, v129, v19\n"
      "v_mfma_f32_32x32x16_bf16 v[112:127], a[76:79], v[144:147], v[112:127]\n"
      "ds_read_b128 a[48:51], v152 offset:1024\n"
      "ds_read_b128 a[52:55], v153 offset:1024\n"
      "v_max3_f32 v173, v32, v33, v34\n"
      "v_max3_f32 v173, v173, v35, v36\n"
      "v_add_f32 v128, v128, v20\n"
      "v_add_f32 v129, v129, v21\n"
      "ds_read_b128 a[56:59], v152 offset:1536\n"
      "ds_read_b128 a[60:63], v153 offset:1536\n"
      "buffer_load_dwordx4 v159, s[28:31], s73 offen offset:1024 nt lds\n"
      "v_add_f32 v128, v128, v22\n"
      "v_add_f32 v129, v129, v23\n"
      "v_mfma_f32_32x32x16_bf16 v[64:79], a[80:83], v[148:151], v[64:79]\n"
      "ds_read_b64_tr_b16 a[64:65], v154 offset:40960\n"
      "ds_read_b64_tr_b16 a[66:67], v155 offset:40960\n"
      "v_max3_f32 v173, v173, v37, v38\n"
      "v_max3_f32 v173, v173, v39, v40\n"
      "v_max3_f32 v173, v173, v41, v42\n"
      "v_add_f32 v128, v128, v24\n"
      "v_mfma_f32_32x32x16_bf16 v[80:95], a[84:87], v[148:151], v[80:95]\n"
      "ds_read_b64_tr_b16 a[68:69], v154 offset:41472\n"
      "ds_read_b64_tr_b16 a[70:71], v155 offset:41472\n"
      "buffer_load_dwordx4 v160, s[28:31], s73 offen offset:2048 nt lds\n"
      "v_add_f32 v129, v129, v25\n"
      "v_add_f32 v128, v128, v26\n"
      "v_mfma_f32_32x32x16_bf16 v[96:111], a[88:91], v[148:151], v[96:111]\n"
      "ds_read_b64_tr_b16 a[72:73], v154 offset:41984\n"
      "ds_read_b64_tr_b16 a[74:75], v155 offset:41984\n"
      "v_max3_f32 v173, v173, v43, v44\n"
      "v_max3_f32 v173, v173, v45, v46\n"
      "v_max_f32 v173, v173, v47\n"
      "v_add_f32 v129, v129, v27\n"
      "v_mfma_f32_32x32x16_bf16 v[112:127], a[92:95], v[148:151], v[112:127]\n"
      "ds_read_b64_tr_b16 a[76:77], v154 offset:42496\n"
      "ds_read_b64_tr_b16 a[78:79], v155 offset:42496\n"
      "buffer_load_dwordx4 v161, s[28:31], s73 offen offset:3072 nt lds\n"
      "v_cmp_lt_f32 vcc, s92, v173\n"
      "v_add_f32 v128, v128, v28\n"
      "v_add_f32 v129, v129, v29\n"
      "v_add_f32 v128, v128, v30\n"
      "v_add_f32 v129, v129, v31\n"
      "s_cbranch_vccnz .Lgq_bk0_rare\n"
      ".Lgq_bk0_ret:\n"
      "s_waitcnt lgkmcnt(8)\n"
      "v_mfma_f32_32x32x16_bf16 v[16:31], a[32:35], v[176:179], v[48:63]\n"
      "ds_read_b64_tr_b16 a[80:81], v154 offset:45056\n"
      "v_exp_f32 v32, v32\n"
      "v_exp_f32 v33, v33\n"
      "v_mfma_f32_32x32x16_bf16 v[16:31], a[36:39], v[180:183], v[16:31]\n"
      "ds_read_b64_tr_b16 a[82:83], v155 offset:45056\n"
      "v_exp_f32 v34, v34\n"
      "v_exp_f32 v35, v35\n"
      "v_cvt_pk_bf16_f32 v144, v32, v33\n"
      "v_mfma_f32_32x32x16_bf16 v[16:31], a[40:43], v[184:187], v[16:31]\n"
      "ds_read_b64_tr_b16 a[84:85], v154 offset:45568\n"
      "v_exp_f32 v36, v36\n"
      "v_exp_f32 v37, v37\n"
      "v_cvt_pk_bf16_f32 v145, v34, v35\n"
      "v_mfma_f32_32x32x16_bf16 v[16:31], a[44:47], v[188:191], v[16:31]\n"
      "ds_read_b64_tr_b16 a[86:87], v155 offset:45568\n"
      "v_exp_f32 v38, v38\n"
      "v_exp_f32 v39, v39\n"
      "v_cvt_pk_bf16_f32 v146, v36, v37\n"
      "v_mfma_f32_32x32x16_bf16 v[16:31], a[48:51], v[192:195], v[16:31]\n"
      "ds_read_b64_tr_b16 a[88:89], v154 offset:46080\n"
      "v_exp_f32 v40, v40\n"
      "v_exp_f32 v41, v41\n"
      "v_cvt_pk_bf16_f32 v147, v38, v39\n"
      "v_mfma_f32_32x32x16_bf16 v[16:31], a[52:55], v[196:199], v[16:31]\n"
      "ds_read_b64_tr_b16 a[90:91], v155 offset:46080\n"
      "v_exp_f32 v42, v42\n"
      "v_exp_f32 v43, v43\n"
      "v_cvt_pk_bf16_f32 v148, v40, v41\n"
      "v_mfma_f32_32x32x16_bf16 v[16:31], a[56:59], v[200:203], v[16:31]\n"
      "ds_read_b64_tr_b16 a[92:93], v154 offset:46592\n"
      "v_exp_f32 v44, v44\n"
      "v_exp_f32 v45, v45\n"
      "v_cvt_pk_bf16_f32 v149, v42, v43\n"
      "v_mfma_f32_32x32x16_bf16 v[16:31], a[60:63], v[204:207], v[16:31]\n"
      "ds_read_b64_tr_b16 a[94:95], v155 offset:46592\n"
      "v_exp_f32 v46, v46\n"
      "v_exp_f32 v47, v47\n"
      "v_cvt_pk_bf16_f32 v150, v44, v45\n"
      "v_cvt_pk_bf16_f32 v151, v46, v47\n"
      "s_waitcnt lgkmcnt(8)\n"
      "v_mfma_f32_32x32x16_bf16 v[64:79], a[64:67], v[144:147], v[64:79]\n"
      "v_add_u32 v154, s76, v154\n"
      "v_add_u32 v155, s76, v155\n"
      "v_add_u32 v156, s76, v156\n"
      "v_add_u32 v157, s76, v157\n"
      "v_add_f32 v128, v128, v32\n"
      "v_mfma_f32_32x32x16_bf16 v[80:95], a[68:71], v[144:147], v[80:95]\n"
      "s_waitcnt vmcnt(4) lgkmcnt(0)\n"
      "s_barrier\n"
      "ds_read_b128 a[32:35], v152 offset:8192\n"
      "ds_read_b128 a[36:39], v153 offset:8192\n"
      "s_add_u32 m0, s72, 32768\n"
      "v_add_f32 v129, v129, v33\n"
      "v_add_f32 v128, v128, v34\n"
      "v_mfma_f32_32x32x16_bf16 v[96:111], a[72:75], v[144:147], v[96:111]\n"
      "ds_read_b128 a[40:43], v152 offset:8704\n"
      "ds_read_b128 a[44:47], v153 offset:8704\n"
      "buffer_load_dwordx4 v158, s[32:35], s73 offen offset:0 nt lds\n"
      "v_add_f32 v129, v129, v35\n"
      "v_mfma_f32_32x32x16_bf16 v[112:127], a[76:79], v[144:147], v[112:127]\n"
      "ds_read_b128 a[48:51], v152 offset:9216\n"
      "ds_read_b128 a[52:55], v153 offset:9216\n"
      "v_max3_f32 v173, v16, v17, v18\n"
      "v_max3_f32 v173, v173, v19, v20\n"
      "v_add_f32 v128, v128, v36\n"
      "v_add_f32 v129, v129, v37\n"
      "ds_read_b128 a[56:59], v152 offset:9728\n"
      "ds_read_b128 a[60:63], v153 offset:9728\n"
      "buffer_load_dwordx4 v159, s[32:35], s73 offen offset:1024 nt lds\n"
      "v_add_f32 v128, v128, v38\n"
      "v_add_f32 v129, v129, v39\n"
      "v_mfma_f32_32x32x16_bf16 v[64:79], a[80:83], v[148:151], v[64:79]\n"
      "s_swappc_b64 s[50:51], s[52:53]\n"
      "v_max3_f32 v173, v173, v21, v22\n"
      "v_max3_f32 v173, v173, v23, v24\n"
      "v_max3_f32 v173, v173, v25, v26\n"
      "v_add_f32 v128, v128, v40\n"
      "v_mfma_f32_32x32x16_bf16 v[80:95], a[84:87], v[148:151], v[80:95]\n"
      "s_waitcnt lgkmcnt(8)\n"
      "ds_read_b64_tr_b16 a[64:65], v154 offset:32768\n"
      "ds_read_b64_tr_b16 a[66:67], v155 offset:32768\n"
      "buffer_load_dwordx4 v160, s[32:35], s73 offen offset:2048 nt lds\n"
      "v_add_f32 v129, v129, v41\n"
      "v_add_f32 v128, v128, v42\n"
      "v_mfma_f32_32x32x16_bf16 v[96:111], a[88:91], v[148:151], v[96:111]\n"
      "ds_read_b64_tr_b16 a[68:69], v154 offset:33280\n"
      "ds_read_b64_tr_b16 a[70:71], v155 offset:33280\n"
      "v_max3_f32 v173, v173, v27, v28\n"
      "v_max3_f32 v173, v173, v29, v30\n"
      "v_max_f32 v173, v173, v31\n"
      "v_add_f32 v129, v129, v43\n"
      "v_mfma_f32_32x32x16_bf16 v[112:127], a[92:95], v[148:151], v[112:127]\n"
      "ds_read_b64_tr_b16 a[72:73], v154 offset:33792\n"
      "ds_read_b64_tr_b16 a[74:75], v155 offset:33792\n"
      "buffer_load_dwordx4 v161, s[32:35], s73 offen offset:3072 nt lds\n"
      "v_cmp_lt_f32 vcc, s92, v173\n"
      "v_add_f32 v128, v128, v44\n"
      "v_add_f32 v129, v129, v45\n"
      "ds_read_b64_tr_b16 a[76:77], v154 offset:34304\n"
      "ds_read_b64_tr_b16 a[78:79], v155 offset:34304\n"
      "s_add_u32 s73, s73, 16384\n"
      "s_add_u32 s74, s74, 0x20000\n"
      "s_and_b32 s74, s74, 0x1fffff\n"
      "s_add_u32 s72, s72, 49152\n"
      "s_cmp_ge_u32 s72, 147456\n"
      "s_cselect_b32 s99, 147456, 0\n"
      "s_sub_u32 s72, s72, s99\n"
      "s_add_u32 s52, s52, 72\n"
      "s_addc_u32 s53, s53, 0\n"
      "s_add_u32 s75, s75, 1\n"
      "v_add_f32 v128, v128, v46\n"
      "v_add_f32 v129, v129, v47\n"
      "s_cbranch_vccnz .Lgq_bk1_rare\n"
      ".Lgq_bk1_ret:\n"
      "s_cmp_lg_u32 s75, 15\n"
      "s_cbranch_scc1 .Lgq_bk_loop\n"
      ".Lgq_bk_skip:\n"
      "s_waitcnt lgkmcnt(8)\n"
      "v_mfma_f32_32x32x16_bf16 v[32:47], a[32:35], v[176:179], v[48:63]\n"
      "ds_read_b64_tr_b16 a[80:81], v154 offset:36864\n"
      "v_exp_f32 v16, v16\n"
      "v_exp_f32 v17, v17\n"
      "v_mfma_f32_32x32x16_bf16 v[32:47], a[36:39], v[180:183], v[32:47]\n"
      "ds_read_b64_tr_b16 a[82:83], v155 offset:36864\n"
      "v_exp_f32 v18, v18\n"
      "v_exp_f32 v19, v19\n"
      "v_cvt_pk_bf16_f32 v144, v16, v17\n"
      "v_mfma_f32_32x32x16_bf16 v[32:47], a[40:43], v[184:187], v[32:47]\n"
      "ds_read_b64_tr_b16 a[84:85], v154 offset:37376\n"
      "v_exp_f32 v20, v20\n"
      "v_exp_f32 v21, v21\n"
      "v_cvt_pk_bf16_f32 v145, v18, v19\n"
      "v_mfma_f32_32x32x16_bf16 v[32:47], a[44:47], v[188:191], v[32:47]\n"
      "ds_read_b64_tr_b16 a[86:87], v155 offset:37376\n"
      "v_exp_f32 v22, v22\n"
      "v_exp_f32 v23, v23\n"
      "v_cvt_pk_bf16_f32 v146, v20, v21\n"
      "v_mfma_f32_32x32x16_bf16 v[32:47], a[48:51], v[192:195], v[32:47]\n"
      "ds_read_b64_tr_b16 a[88:89], v154 offset:37888\n"
      "v_exp_f32 v24, v24\n"
      "v_exp_f32 v25, v25\n"
      "v_cvt_pk_bf16_f32 v147, v22, v23\n"
      "v_mfma_f32_32x32x16_bf16 v[32:47], a[52:55], v[196:199], v[32:47]\n"
      "ds_read_b64_tr_b16 a[90:91], v155 offset:37888\n"
      "v_exp_f32 v26, v26\n"
      "v_exp_f32 v27, v27\n"
      "v_cvt_pk_bf16_f32 v148, v24, v25\n"
      "v_mfma_f32_32x32x16_bf16 v[32:47], a[56:59], v[200:203], v[32:47]\n"
      "ds_read_b64_tr_b16 a[92:93], v154 offset:38400\n"
      "v_exp_f32 v28, v28\n"
      "v_exp_f32 v29, v29\n"
      "v_cvt_pk_bf16_f32 v149, v26, v27\n"
      "v_mfma_f32_32x32x16_bf16 v[32:47], a[60:63], v[204:207], v[32:47]\n"
      "ds_read_b64_tr_b16 a[94:95], v155 offset:38400\n"
      "v_exp_f32 v30, v30\n"
      "v_exp_f32 v31, v31\n"
      "v_cvt_pk_bf16_f32 v150, v28, v29\n"
      "v_cvt_pk_bf16_f32 v151, v30, v31\n"
      "s_waitcnt lgkmcnt(8)\n"
      "v_mfma_f32_32x32x16_bf16 v[64:79], a[64:67], v[144:147], v[64:79]\n"
      "s_cmp_eq_u32 s77, 98304\n"
      "s_cselect_b32 s76, 4294868992, s78\n"
      "s_add_u32 s77, s77, s76\n"
      "v_add_u32 v152, s76, v152\n"
      "v_add_u32 v153, s76, v153\n"
      "v_add_f32 v128, v128, v16\n"
      "v_mfma_f32_32x32x16_bf16 v[80:95], a[68:71], v[144:147], v[80:95]\n"
      "s_waitcnt vmcnt(4) lgkmcnt(0)\n"
      "s_barrier\n"
      "ds_read_b128 a[32:35], v152 offset:0\n"
      "ds_read_b128 a[36:39], v153 offset:0\n"
      "v_add_f32 v129, v129, v17\n"
      "v_add_f32 v128, v128, v18\n"
      "v_mfma_f32_32x32x16_bf16 v[96:111], a[72:75], v[144:147], v[96:111]\n"
      "ds_read_b128 a[40:43], v152 offset:512\n"
      "ds_read_b128 a[44:47], v153 offset:512\n"
      "v_add_f32 v129, v129, v19\n"
      "v_mfma_f32_32x32x16_bf16 v[112:127], a[76:79], v[144:147], v[112:127]\n"
      "ds_read_b128 a[48:51], v152 offset:1024\n"
      "ds_read_b128 a[52:55], v153 offset:1024\n"
      "v_max3_f32 v173, v32, v33, v34\n"
      "v_max3_f32 v173, v173, v35, v36\n"
      "v_add_f32 v128, v128, v20\n"
      "v_add_f32 v129, v129, v21\n"
      "ds_read_b128 a[56:59], v152 offset:1536\n"
      "ds_read_b128 a[60:63], v153 offset:1536\n"
      "v_add_f32 v128, v128, v22\n"
      "v_add_f32 v129, v129, v23\n"
      "v_mfma_f32_32x32x16_bf16 v[64:79], a[80:83], v[148:151], v[64:79]\n"
      "ds_read_b64_tr_b16 a[64:65], v154 offset:40960\n"
      "ds_read_b64_tr_b16 a[66:67], v155 offset:40960\n"
      "v_max3_f32 v173, v173, v37, v38\n"
      "v_max3_f32 v173, v173, v39, v40\n"
      "v_max3_f32 v173, v173, v41, v42\n"
      "v_add_f32 v128, v128, v24\n"
      "v_mfma_f32_32x32x16_bf16 v[80:95], a[84:87], v[148:151], v[80:95]\n"
      "ds_read_b64_tr_b16 a[68:69], v154 offset:41472\n"
      "ds_read_b64_tr_b16 a[70:71], v155 offset:41472\n"
      "v_add_f32 v129, v129, v25\n"
      "v_add_f32 v128, v128, v26\n"
      "v_mfma_f32_32x32x16_bf16 v[96:111], a[88:91], v[148:151], v[96:111]\n"
      "ds_read_b64_tr_b16 a[72:73], v154 offset:41984\n"
      "ds_read_b64_tr_b16 a[74:75], v155 offset:41984\n"
      "v_max3_f32 v173, v173, v43, v44\n"
      "v_max3_f32 v173, v173, v45, v46\n"
      "v_max_f32 v173, v173, v47\n"
      "v_add_f32 v129, v129, v27\n"
      "v_mfma_f32_32x32x16_bf16 v[112:127], a[92:95], v[148:151], v[112:127]\n"
      "ds_read_b64_tr_b16 a[76:77], v154 offset:42496\n"
      "ds_read_b64_tr_b16 a[78:79], v155 offset:42496\n"
      "v_cmp_lt_f32 vcc, s92, v173\n"
      "v_add_f32 v128, v128, v28\n"
      "v_add_f32 v129, v129, v29\n"
      "v_add_f32 v128, v128, v30\n"
      "v_add_f32 v129, v129, v31\n"
      "s_cbranch_vccnz .Lgq_bp0_rare\n"
      ".Lgq_bp0_ret:\n"
      "s_waitcnt lgkmcnt(8)\n"
      "v_mfma_f32_32x32x16_bf16 v[16:31], a[32:35], v[176:179], v[48:63]\n"
      "ds_read_b64_tr_b16 a[80:81], v154 offset:45056\n"
      "v_exp_f32 v32, v32\n"
      "v_exp_f32 v33, v33\n"
      "v_mfma_f32_32x32x16_bf16 v[16:31], a[36:39], v[180:183], v[16:31]\n"
      "ds_read_b64_tr_b16 a[82:83], v155 offset:45056\n"
      "v_exp_f32 v34, v34\n"
      "v_exp_f32 v35, v35\n"
      "v_cvt_pk_bf16_f32 v144, v32, v33\n"
      "v_mfma_f32_32x32x16_bf16 v[16:31], a[40:43], v[184:187], v[16:31]\n"
      "ds_read_b64_tr_b16 a[84:85], v154 offset:45568\n"
      "v_exp_f32 v36, v36\n"
      "v_exp_f32 v37, v37\n"
      "v_cvt_pk_bf16_f32 v145, v34, v35\n"
      "v_mfma_f32_32x32x16_bf16 v[16:31], a[44:47], v[188:191], v[16:31]\n"
      "ds_read_b64_tr_b16 a[86:87], v155 offset:45568\n"
      "v_exp_f32 v38, v38\n"
      "v_exp_f32 v39, v39\n"
      "v_cvt_pk_bf16_f32 v146, v36, v37\n"
      "v_mfma_f32_32x32x16_bf16 v[16:31], a[48:51], v[192:195], v[16:31]\n"
      "ds_read_b64_tr_b16 a[88:89], v154 offset:46080\n"
      "v_exp_f32 v40, v40\n"
      "v_exp_f32 v41, v41\n"
      "v_cvt_pk_bf16_f32 v147, v38, v39\n"
      "v_mfma_f32_32x32x16_bf16 v[16:31], a[52:55], v[196:199], v[16:31]\n"
      "ds_read_b64_tr_b16 a[90:91], v155 offset:46080\n"
      "v_exp_f32 v42, v42\n"
      "v_exp_f32 v43, v43\n"
      "v_cvt_pk_bf16_f32 v148, v40, v41\n"
      "v_mfma_f32_32x32x16_bf16 v[16:31], a[56:59], v[200:203], v[16:31]\n"
      "ds_read_b64_tr_b16 a[92:93], v154 offset:46592\n"
      "v_exp_f32 v44, v44\n"
      "v_exp_f32 v45, v45\n"
      "v_cvt_pk_bf16_f32 v149, v42, v43\n"
      "v_mfma_f32_32x32x16_bf16 v[16:31], a[60:63], v[204:207], v[16:31]\n"
      "ds_read_b64_tr_b16 a[94:95], v155 offset:46592\n"
      "v_exp_f32 v46, v46\n"
      "v_exp_f32 v47, v47\n"
      "v_cvt_pk_bf16_f32 v150, v44, v45\n"
      "v_cvt_pk_bf16_f32 v151, v46, v47\n"
      "s_waitcnt lgkmcnt(8)\n"
      "v_mfma_f32_32x32x16_bf16 v[64:79], a[64:67], v[144:147], v[64:79]\n"
      "v_add_u32 v154, s76, v154\n"
      "v_add_u32 v155, s76, v155\n"
      "v_add_u32 v156, s76, v156\n"
      "v_add_u32 v157, s76, v157\n"
      "v_add_f32 v128, v128, v32\n"
      "v_mfma_f32_32x32x16_bf16 v[80:95], a[68:71], v[144:147], v[80:95]\n"
      "s_waitcnt vmcnt(0) lgkmcnt(0)\n"
      "s_barrier\n"
      "ds_read_b128 a[32:35], v152 offset:8192\n"
      "ds_read_b128 a[36:39], v153 offset:8192\n"
      "v_add_f32 v129, v129, v33\n"
      "v_add_f32 v128, v128, v34\n"
      "v_mfma_f32_32x32x16_bf16 v[96:111], a[72:75], v[144:147], v[96:111]\n"
      "ds_read_b128 a[40:43], v152 offset:8704\n"
      "ds_read_b128 a[44:47], v153 offset:8704\n"
      "s_cmp_lg_u32 s79, 0\n"
      "s_cbranch_scc1 .Lgq_bp_nox\n"
      "buffer_load_dword v13, off, s[40:43], s57 offset:2060 sc1\n"
      ".Lgq_bp_nox:\n"
      "v_add_f32 v129, v129, v35\n"
      "v_mfma_f32_32x32x16_bf16 v[112:127], a[76:79], v[144:147], v[112:127]\n"
      "ds_read_b128 a[48:51], v152 offset:9216\n"
      "ds_read_b128 a[52:55], v153 offset:9216\n"
      "v_max3_f32 v173, v16, v17, v18\n"
      "v_max3_f32 v173, v173, v19, v20\n"
      "v_add_f32 v128, v128, v36\n"
      "v_add_f32 v129, v129, v37\n"
      "ds_read_b128 a[56:59], v152 offset:9728\n"
      "ds_read_b128 a[60:63], v153 offset:9728\n"
      "v_add_f32 v128, v128, v38\n"
      "v_add_f32 v129, v129, v39\n"
      "v_mfma_f32_32x32x16_bf16 v[64:79], a[80:83], v[148:151], v[64:79]\n"
      "s_swappc_b64 s[50:51], s[52:53]\n"
      "v_max3_f32 v173, v173, v21, v22\n"
      "v_max3_f32 v173, v173, v23, v24\n"
      "v_max3_f32 v173, v173, v25, v26\n"
      "v_add_f32 v128, v128, v40\n"
      "v_mfma_f32_32x32x16_bf16 v[80:95], a[84:87], v[148:151], v[80:95]\n"
      "s_waitcnt lgkmcnt(8)\n"
      "ds_read_b64_tr_b16 a[64:65], v154 offset:32768\n"
      "ds_read_b64_tr_b16 a[66:67], v155 offset:32768\n"
      "v_add_f32 v129, v129, v41\n"
      "v_add_f32 v128, v128, v42\n"
      "v_mfma_f32_32x32x16_bf16 v[96:111], a[88:91], v[148:151], v[96:111]\n"
      "ds_read_b64_tr_b16 a[68:69], v154 offset:33280\n"
      "ds_read_b64_tr_b16 a[70:71], v155 offset:33280\n"
      "v_max3_f32 v173, v173, v27, v28\n"
      "v_max3_f32 v173, v173, v29, v30\n"
      "v_max_f32 v173, v173, v31\n"
      "v_add_f32 v129, v129, v43\n"
      "v_mfma_f32_32x32x16_bf16 v[112:127], a[92:95], v[148:151], v[112:127]\n"
      "ds_read_b64_tr_b16 a[72:73], v154 offset:33792\n"
      "ds_read_b64_tr_b16 a[74:75], v155 offset:33792\n"
      "v_cmp_lt_f32 vcc, s92, v173\n"
      "v_add_f32 v128, v128, v44\n"
      "v_add_f32 v129, v129, v45\n"
      "ds_read_b64_tr_b16 a[76:77], v154 offset:34304\n"
      "ds_read_b64_tr_b16 a[78:79], v155 offset:34304\n"
      "s_add_u32 s52, s52, 72\n"
      "s_addc_u32 s53, s53, 0\n"
      "v_add_f32 v128, v128, v46\n"
      "v_add_f32 v129, v129, v47\n"
      "s_cbranch_vccnz .Lgq_bp1_rare\n"
      ".Lgq_bp1_ret:\n"
      "s_cmp_eq_u32 s79, 0\n"
      "s_cbranch_scc1 .Lgq_wx_skip\n"
      "s_add_u32 m0, s71, 114688\n"
      "s_nop 0\n"
      "buffer_load_dwordx4 v162, s[36:39], s74 offen offset:0  lds\n"
      "buffer_load_dwordx4 v163, s[36:39], s74 offen offset:1024  lds\n"
      "buffer_load_dwordx4 v164, s[36:39], s74 offen offset:2048  lds\n"
      "buffer_load_dwordx4 v165, s[36:39], s74 offen offset:3072  lds\n"
      "s_add_u32 s74, s74, 0x20000\n"
      "s_and_b32 s74, s74, 0x1fffff\n"
      ".Lgq_wx_skip:\n"
      "s_waitcnt lgkmcnt(8)\n"
      "v_mfma_f32_32x32x16_bf16 v[32:47], a[32:35], v[176:179], v[48:63]\n"
      "ds_read_b64_tr_b16 a[80:81], v154 offset:36864\n"
      "v_exp_f32 v16, v16\n"
      "v_exp_f32 v17, v17\n"
      "v_mfma_f32_32x32x16_bf16 v[32:47], a[36:39], v[180:183], v[32:47]\n"
      "ds_read_b64_tr_b16 a[82:83], v155 offset:36864\n"
      "v_exp_f32 v18, v18\n"
      "v_exp_f32 v19, v19\n"
      "v_cvt_pk_bf16_f32 v144, v16, v17\n"
      "v_mfma_f32_32x32x16_bf16 v[32:47], a[40:43], v[184:187], v[32:47]\n"
      "ds_read_b64_tr_b16 a[84:85], v154 offset:37376\n"
      "v_exp_f32 v20, v20\n"
      "v_exp_f32 v21, v21\n"
      "v_cvt_pk_bf16_f32 v145, v18, v19\n"
      "v_mfma_f32_32x32x16_bf16 v[32:47], a[44:47], v[188:191], v[32:47]\n"
      "ds_read_b64_tr_b16 a[86:87], v155 offset:37376\n"
      "v_exp_f32 v22, v22\n"
      "v_exp_f32 v23, v23\n"
      "v_cvt_pk_bf16_f32 v146, v20, v21\n"
      "v_mfma_f32_32x32x16_bf16 v[32:47], a[48:51], v[192:195], v[32:47]\n"
      "ds_read_b64_tr_b16 a[88:89], v154 offset:37888\n"
      "v_exp_f32 v24, v24\n"
      "v_exp_f32 v25, v25\n"
      "v_cvt_pk_bf16_f32 v147, v22, v23\n"
      "v_mfma_f32_32x32x16_bf16 v[32:47], a[52:55], v[196:199], v[32:47]\n"
      "ds_read_b64_tr_b16 a[90:91], v155 offset:37888\n"
      "v_exp_f32 v26, v26\n"
      "v_exp_f32 v27, v27\n"
      "v_cvt_pk_bf16_f32 v148, v24, v25\n"
      "v_mfma_f32_32x32x16_bf16 v[32:47], a[56:59], v[200:203], v[32:47]\n"
      "ds_read_b64_tr_b16 a[92:93], v154 offset:38400\n"
      "v_exp_f32 v28, v28\n"
      "v_exp_f32 v29, v29\n"
      "v_cvt_pk_bf16_f32 v149, v26, v27\n"
      "v_mfma_f32_32x32x16_bf16 v[32:47], a[60:63], v[204:207], v[32:47]\n"
      "ds_read_b64_tr_b16 a[94:95], v155 offset:38400\n"
      "v_exp_f32 v30, v30\n"
      "v_exp_f32 v31, v31\n"
      "v_cvt_pk_bf16_f32 v150, v28, v29\n"
      "v_cvt_pk_bf16_f32 v151, v30, v31\n"
      "s_waitcnt lgkmcnt(8)\n"
      "v_mfma_f32_32x32x16_bf16 v[64:79], a[64:67], v[144:147], v[64:79]\n"
      "s_add_u32 s80, s56, 4096\n"
      "s_add_u32 s81, s56, 8192\n"
      "s_mov_b32 s86, 0\n"
      "s_cmp_lg_u32 s79, 0\n"
      "s_cbranch_scc1 .Lgq_xl_skip\n"
      "s_waitcnt vmcnt(0)\n"
      "v_cmp_eq_u32 vcc, s48, v13\n"
      "s_nop 1\n"
      "s_cmp_eq_u64 vcc, exec\n"
      "s_cbranch_scc0 .Lgq_xl_done\n"
      "s_mov_b32 s86, 1\n"
      "buffer_load_dwordx4 v[176:179], v170, s[40:43], s56 offen offset:0 sc1\n"
      "buffer_load_dwordx4 v[180:183], v170, s[40:43], s56 offen offset:1024 sc1\n"
      "buffer_load_dwordx4 v[184:187], v170, s[40:43], s56 offen offset:2048 sc1\n"
      "buffer_load_dwordx4 v[188:191], v170, s[40:43], s56 offen offset:3072 sc1\n"
      "buffer_load_dwordx4 v[192:195], v170, s[40:43], s80 offen offset:0 sc1\n"
      "buffer_load_dwordx4 v[196:199], v170, s[40:43], s80 offen offset:1024 sc1\n"
      "buffer_load_dwordx4 v[200:203], v170, s[40:43], s80 offen offset:2048 sc1\n"
      "buffer_load_dwordx4 v[204:207], v170, s[40:43], s80 offen offset:3072 sc1\n"
      "buffer_load_dwordx4 v[132:135], v170, s[40:43], s81 offen offset:0 sc1\n"
      "buffer_load_dwordx4 v[136:139], v170, s[40:43], s81 offen offset:1024 sc1\n"
      "buffer_load_dwordx4 v[140:143], v170, s[40:43], s81 offen offset:2048 sc1\n"
      "s_branch .Lgq_xl_done\n"
      ".Lgq_xl_skip:\n"
      "v_mov_b32 v179, s48\n"
      "v_mov_b32 v183, s48\n"
      "v_mov_b32 v187, s48\n"
      "v_mov_b32 v191, s48\n"
      "v_mov_b32 v195, s48\n"
      "v_mov_b32 v199, s48\n"
      "v_mov_b32 v203, s48\n"
      "v_mov_b32 v207, s48\n"
      "v_mov_b32 v135, s48\n"
      "v_mov_b32 v139, s48\n"
      "v_mov_b32 v143, s48\n"
      ".Lgq_xl_done:\n"
      "v_add_f32 v128, v128, v16\n"
      "v_mfma_f32_32x32x16_bf16 v[80:95], a[68:71], v[144:147], v[80:95]\n"
      "v_bfe_u32 v2, v172, 4, 1\n"
      "v_and_b32 v3, 15, v172\n"
      "v_lshlrev_b32 v3, 4, v3\n"
      "v_lshl_add_u32 v1, v2, 12, v3\n"
      "v_add_f32 v129, v129, v17\n"
      "v_add_f32 v128, v128, v18\n"
      "v_mfma_f32_32x32x16_bf16 v[96:111], a[72:75], v[144:147], v[96:111]\n"
      "v_lshrrev_b32 v2, 5, v172\n"
      "v_lshl_add_u32 v1, v2, 8, v1\n"
      "v_max3_f32 v173, v32, v33, v34\n"
      "v_max3_f32 v173, v173, v35, v36\n"
      "v_add_f32 v129, v129, v19\n"
      "v_mfma_f32_32x32x16_bf16 v[112:127], a[76:79], v[144:147], v[112:127]\n"
      "v_max3_f32 v173, v173, v37, v38\n"
      "v_max3_f32 v173, v173, v39, v40\n"
      "v_max3_f32 v173, v173, v41, v42\n"
      "v_max3_f32 v173, v173, v43, v44\n"
      "v_add_f32 v128, v128, v20\n"
      "v_add_f32 v129, v129, v21\n"
      "v_max3_f32 v173, v173, v45, v46\n"
      "v_max_f32 v173, v173, v47\n"
      "v_cmp_lt_f32 vcc, s92, v173\n"
      "s_waitcnt lgkmcnt(0)\n"
      "v_add_f32 v128, v128, v22\n"
      "v_add_f32 v129, v129, v23\n"
      "v_mfma_f32_32x32x16_bf16 v[64:79], a[80:83], v[148:151], v[64:79]\n"
      "ds_read_b64_tr_b16 a[64:65], v154 offset:40960\n"
      "ds_read_b64_tr_b16 a[66:67], v155 offset:40960\n"
      "v_add_f32 v128, v128, v24\n"
      "v_mfma_f32_32x32x16_bf16 v[80:95], a[84:87], v[148:151], v[80:95]\n"
      "ds_read_b64_tr_b16 a[68:69], v154 offset:41472\n"
      "ds_read_b64_tr_b16 a[70:71], v155 offset:41472\n"
      "v_add_f32 v129, v129, v25\n"
      "v_add_f32 v128, v128, v26\n"
      "v_mfma_f32_32x32x16_bf16 v[96:111], a[88:91], v[148:151], v[96:111]\n"
      "ds_read_b64_tr_b16 a[72:73], v154 offset:41984\n"
      "ds_read_b64_tr_b16 a[74:75], v155 offset:41984\n"
      "v_add_f32 v129, v129, v27\n"
      "v_mfma_f32_32x32x16_bf16 v[112:127], a[92:95], v[148:151], v[112:127]\n"
      "ds_read_b64_tr_b16 a[76:77], v154 offset:42496\n"
      "ds_read_b64_tr_b16 a[78:79], v155 offset:42496\n"
      "v_add_f32 v128, v128, v28\n"
      "v_add_f32 v129, v129, v29\n"
      "v_add_f32 v128, v128, v30\n"
      "v_add_f32 v129, v129, v31\n"
      "s_cbranch_vccnz .Lgq_bl_rare\n"
      ".Lgq_bl_ret:\n"
      "s_waitcnt lgkmcnt(8)\n"
      "ds_read_b64_tr_b16 a[80:81], v154 offset:45056\n"
      "v_exp_f32 v32, v32\n"
      "v_exp_f32 v33, v33\n"
      "ds_read_b64_tr_b16 a[82:83], v155 offset:45056\n"
      "v_exp_f32 v34, v34\n"
      "v_exp_f32 v35, v35\n"
      "v_cvt_pk_bf16_f32 v144, v32, v33\n"
      "ds_read_b64_tr_b16 a[84:85], v154 offset:45568\n"
      "v_exp_f32 v36, v36\n"
      "v_exp_f32 v37, v37\n"
      "v_cvt_pk_bf16_f32 v145, v34, v35\n"
      "ds_read_b64_tr_b16 a[86:87], v155 offset:45568\n"
      "v_exp_f32 v38, v38\n"
      "v_exp_f32 v39, v39\n"
      "v_cvt_pk_bf16_f32 v146, v36, v37\n"
      "ds_read_b64_tr_b16 a[88:89], v154 offset:46080\n"
      "v_exp_f32 v40, v40\n"
      "v_exp_f32 v41, v41\n"
      "v_cvt_pk_bf16_f32 v147, v38, v39\n"
      "ds_read_b64_tr_b16 a[90:91], v155 offset:46080\n"
      "v_exp_f32 v42, v42\n"
      "v_exp_f32 v43, v43\n"
      "v_cvt_pk_bf16_f32 v148, v40, v41\n"
      "ds_read_b64_tr_b16 a[92:93], v154 offset:46592\n"
      "v_exp_f32 v44, v44\n"
      "v_exp_f32 v45, v45\n"
      "v_cvt_pk_bf16_f32 v149, v42, v43\n"
      "ds_read_b64_tr_b16 a[94:95], v155 offset:46592\n"
      "v_exp_f32 v46, v46\n"
      "v_exp_f32 v47, v47\n"
      "v_cvt_pk_bf16_f32 v150, v44, v45\n"
      "v_cvt_pk_bf16_f32 v151, v46, v47\n"
      "s_cmp_lg_u32 s79, 0\n"
      "s_cbranch_scc1 .Lgq_e_epi\n"
      "s_cmp_eq_u32 s86, 0\n"
      "s_cbranch_scc1 .Lgq_x_hint\n"
      ".Lgq_x_have:\n"
      "s_waitcnt lgkmcnt(8)\n"
      "v_mfma_f32_32x32x16_bf16 v[64:79], a[64:67], v[144:147], v[64:79]\n"
      "v_add_f32 v128, v128, v32\n"
      "v_add_f32 v129, v129, v33\n"
      "v_add_f32 v128, v128, v34\n"
      "v_add_f32 v129, v129, v35\n"
      "v_add_f32 v128, v128, v36\n"
      "v_add_f32 v129, v129, v37\n"
      "v_mfma_f32_32x32x16_bf16 v[80:95], a[68:71], v[144:147], v[80:95]\n"
      "v_add_f32 v128, v128, v38\n"
      "v_add_f32 v129, v129, v39\n"
      "v_add_f32 v128, v128, v40\n"
      "v_add_f32 v129, v129, v41\n"
      "v_add_f32 v128, v128, v42\n"
      "v_add_f32 v129, v129, v43\n"
      "s_waitcnt lgkmcnt(0)\n"
      "v_mfma_f32_32x32x16_bf16 v[64:79], a[80:83], v[148:151], v[64:79]\n"
      "v_add_f32 v128, v128, v44\n"
      "v_add_f32 v129, v129, v45\n"
      "v_add_f32 v128, v128, v46\n"
      "v_add_f32 v129, v129, v47\n"
      "v_add_f32 v6, v128, v129\n"
      "v_mov_b32 v7, v6\n"
      "v_mfma_f32_32x32x16_bf16 v[80:95], a[84:87], v[148:151], v[80:95]\n"
      "s_nop 0\n"
      "v_permlane32_swap_b32 v6, v7\n"
      "v_add_f32 v7, v6, v7\n"
      "v_rcp_f32 v6, v7\n"
      "v_log_f32 v7, v7\n"
      "v_mfma_f32_32x32x16_bf16 v[96:111], a[72:75], v[144:147], v[96:111]\n"
      "v_sub_f32 v7, v7, v48\n"
      "s_waitcnt vmcnt(0)\n"
      "v_min3_u32 v2, v179, v183, v187\n"
      "v_max3_u32 v3, v179, v183, v187\n"
      "v_min3_u32 v2, v2, v191, v195\n"
      "v_max3_u32 v3, v3, v191, v195\n"
      "v_min3_u32 v2, v2, v199, v203\n"
      "v_max3_u32 v3, v3, v199, v203\n"
      "v_mfma_f32_32x32x16_bf16 v[96:111], a[88:91], v[148:151], v[96:111]\n"
      "v_min3_u32 v2, v2, v207, v135\n"
      "v_max3_u32 v3, v3, v207, v135\n"
      "v_min3_u32 v2, v2, v139, v143\n"
      "v_max3_u32 v3, v3, v139, v143\n"
      "s_mov_b32 s54, s48\n"
      "s_mov_b32 s55, s48\n"
      "v_cmp_eq_u64 vcc, s[54:55], v[2:3]\n"
      "s_nop 1\n"
      "s_cmp_eq_u64 vcc, exec\n"
      "s_cbranch_scc0 .Lgq_x_slow\n"
      ".Lgq_x_go:\n"
      "v_mfma_f32_32x32x16_bf16 v[112:127], a[76:79], v[144:147], v[112:127]\n"
      "v_lshlrev_b32 v16, 16, v176\n"
      "v_and_b32 v17, 0xffff0000, v176\n"
      "v_lshlrev_b32 v18, 16, v177\n"
      "v_and_b32 v19, 0xffff0000, v177\n"
      "v_lshlrev_b32 v20, 16, v178\n"
      "v_and_b32 v21, 0xffff0000, v178\n"
      "v_mfma_f32_32x32x16_bf16 v[112:127], a[92:95], v[148:151], v[112:127]\n"
      "v_sub_f32 v8, v7, v142\n"
      "v_exp_f32 v8, v8\n"
      "v_lshlrev_b32 v22, 16, v180\n"
      "v_add_f32 v8, 1.0, v8\n"
      "v_rcp_f32 v8, v8\n"
      "v_and_b32 v23, 0xffff0000, v180\n"
      "v_sub_f32 v9, 1.0, v8\n"
      "v_mul_f32 v6, v9, v6\n"
      "v_lshlrev_b32 v24, 16, v181\n"
      "v_and_b32 v25, 0xffff0000, v181\n"
      "v_lshlrev_b32 v26, 16, v182\n"
      "v_and_b32 v27, 0xffff0000, v182\n"
      "v_lshlrev_b32 v28, 16, v184\n"
      "v_and_b32 v29, 0xffff0000, v184\n"
      "v_lshlrev_b32 v30, 16, v185\n"
      "v_and_b32 v31, 0xffff0000, v185\n"
      "v_lshlrev_b32 v50, 16, v186\n"
      "v_and_b32 v51, 0xffff0000, v186\n"
      "v_lshlrev_b32 v52, 16, v188\n"
      "v_and_b32 v53, 0xffff0000, v188\n"
      "v_lshlrev_b32 v54, 16, v189\n"
      "v_and_b32 v55, 0xffff0000, v189\n"
      "v_lshlrev_b32 v56, 16, v190\n"
      "v_and_b32 v57, 0xffff0000, v190\n"
      "v_lshlrev_b32 v58, 16, v192\n"
      "v_and_b32 v59, 0xffff0000, v192\n"
      "v_lshlrev_b32 v60, 16, v193\n"
      "v_and_b32 v61, 0xffff0000, v193\n"
      "v_lshlrev_b32 v62, 16, v194\n"
      "v_and_b32 v63, 0xffff0000, v194\n"
      "v_lshlrev_b32 v32, 16, v196\n"
      "v_and_b32 v33, 0xffff0000, v196\n"
      "v_lshlrev_b32 v34, 16, v197\n"
      "v_and_b32 v35, 0xffff0000, v197\n"
      "v_lshlrev_b32 v36, 16, v198\n"
      "v_and_b32 v37, 0xffff0000, v198\n"
      "v_lshlrev_b32 v38, 16, v200\n"
      "v_and_b32 v39, 0xffff0000, v200\n"
      "v_lshlrev_b32 v40, 16, v201\n"
      "v_and_b32 v41, 0xffff0000, v201\n"
      "v_lshlrev_b32 v42, 16, v202\n"
      "v_and_b32 v43, 0xffff0000, v202\n"
      "v_lshlrev_b32 v44, 16, v204\n"
      "v_and_b32 v45, 0xffff0000, v204\n"
      "v_lshlrev_b32 v46, 16, v205\n"
      "v_and_b32 v47, 0xffff0000, v205\n"
      "v_pk_mul_f32 v[64:65], v[64:65], v[6:7] op_sel_hi:[1,0]\n"
      "v_pk_mul_f32 v[66:67], v[66:67], v[6:7] op_sel_hi:[1,0]\n"
      "v_pk_mul_f32 v[68:69], v[68:69], v[6:7] op_sel_hi:[1,0]\n"
      "v_pk_mul_f32 v[70:71], v[70:71], v[6:7] op_sel_hi:[1,0]\n"
      "v_pk_fma_f32 v[64:65], v[16:17], v[8:9], v[64:65] op_sel_hi:[1,0,1]\n"
      "v_pk_fma_f32 v[66:67], v[18:19], v[8:9], v[66:67] op_sel_hi:[1,0,1]\n"
      "v_pk_fma_f32 v[68:69], v[20:21], v[8:9], v[68:69] op_sel_hi:[1,0,1]\n"
      "v_pk_fma_f32 v[70:71], v[22:23], v[8:9], v[70:71] op_sel_hi:[1,0,1]\n"
      "v_cvt_pk_bf16_f32 v64, v64, v65\n"
      "v_cvt_pk_bf16_f32 v65, v66, v67\n"
      "v_cvt_pk_bf16_f32 v66, v68, v69\n"
      "v_cvt_pk_bf16_f32 v67, v70, v71\n"
      "buffer_store_dwordx4 v[64:67], v1, s[40:43], s59 offen offset:0 sc1\n"
      "v_pk_mul_f32 v[72:73], v[72:73], v[6:7] op_sel_hi:[1,0]\n"
      "v_pk_mul_f32 v[74:75], v[74:75], v[6:7] op_sel_hi:[1,0]\n"
      "v_pk_mul_f32 v[76:77], v[76:77], v[6:7] op_sel_hi:[1,0]\n"
      "v_pk_mul_f32 v[78:79], v[78:79], v[6:7] op_sel_hi:[1,0]\n"
      "v_pk_fma_f32 v[72:73], v[24:25], v[8:9], v[72:73] op_sel_hi:[1,0,1]\n"
      "v_pk_fma_f32 v[74:75], v[26:27], v[8:9], v[74:75] op_sel_hi:[1,0,1]\n"
      "v_pk_fma_f32 v[76:77], v[28:29], v[8:9], v[76:77] op_sel_hi:[1,0,1]\n"
      "v_pk_fma_f32 v[78:79], v[30:31], v[8:9], v[78:79] op_sel_hi:[1,0,1]\n"
      "v_cvt_pk_bf16_f32 v72, v72, v73\n"
      "v_cvt_pk_bf16_f32 v73, v74, v75\n"
      "v_cvt_pk_bf16_f32 v74, v76, v77\n"
      "v_cvt_pk_bf16_f32 v75, v78, v79\n"
      "buffer_store_dwordx4 v[72:75], v1, s[40:43], s59 offen offset:512 sc1\n"
      "v_pk_mul_f32 v[80:81], v[80:81], v[6:7] op_sel_hi:[1,0]\n"
      "v_pk_mul_f32 v[82:83], v[82:83], v[6:7] op_sel_hi:[1,0]\n"
      "v_pk_mul_f32 v[84:85], v[84:85], v[6:7] op_sel_hi:[1,0]\n"
      "v_pk_mul_f32 v[86:87], v[86:87], v[6:7] op_sel_hi:[1,0]\n"
      "v_pk_fma_f32 v[80:81], v[50:51], v[8:9], v[80:81] op_sel_hi:[1,0,1]\n"
      "v_pk_fma_f32 v[82:83], v[52:53], v[8:9], v[82:83] op_sel_hi:[1,0,1]\n"
      "v_pk_fma_f32 v[84:85], v[54:55], v[8:9], v[84:85] op_sel_hi:[1,0,1]\n"
      "v_pk_fma_f32 v[86:87], v[56:57], v[8:9], v[86:87] op_sel_hi:[1,0,1]\n"
      "v_cvt_pk_bf16_f32 v80, v80, v81\n"
      "v_cvt_pk_bf16_f32 v81, v82, v83\n"
      "v_cvt_pk_bf16_f32 v82, v84, v85\n"
      "v_cvt_pk_bf16_f32 v83, v86, v87\n"
      "buffer_store_dwordx4 v[80:83], v1, s[40:43], s59 offen offset:1024 sc1\n"
      "v_pk_mul_f32 v[88:89], v[88:89], v[6:7] op_sel_hi:[1,0]\n"
      "v_pk_mul_f32 v[90:91], v[90:91], v[6:7] op_sel_hi:[1,0]\n"
      "v_pk_mul_f32 v[92:93], v[92:93], v[6:7] op_sel_hi:[1,0]\n"
      "v_pk_mul_f32 v[94:95], v[94:95], v[6:7] op_sel_hi:[1,0]\n"
      "v_pk_fma_f32 v[88:89], v[58:59], v[8:9], v[88:89] op_sel_hi:[1,0,1]\n"
      "v_pk_fma_f32 v[90:91], v[60:61], v[8:9], v[90:91] op_sel_hi:[1,0,1]\n"
      "v_pk_fma_f32 v[92:93], v[62:63], v[8:9], v[92:93] op_sel_hi:[1,0,1]\n"
      "v_pk_fma_f32 v[94:95], v[32:33], v[8:9], v[94:95] op_sel_hi:[1,0,1]\n"
      "v_cvt_pk_bf16_f32 v88, v88, v89\n"
      "v_cvt_pk_bf16_f32 v89, v90, v91\n"
      "v_cvt_pk_bf16_f32 v90, v92, v93\n"
      "v_cvt_pk_bf16_f32 v91, v94, v95\n"
      "buffer_store_dwordx4 v[88:91], v1, s[40:43], s59 offen offset:1536 sc1\n"
      "v_lshlrev_b32 v16, 16, v206\n"
      "v_and_b32 v17, 0xffff0000, v206\n"
      "v_lshlrev_b32 v18, 16, v132\n"
      "v_and_b32 v19, 0xffff0000, v132\n"
      "v_lshlrev_b32 v20, 16, v133\n"
      "v_and_b32 v21, 0xffff0000, v133\n"
      "v_lshlrev_b32 v22, 16, v134\n"
      "v_and_b32 v23, 0xffff0000, v134\n"
      "v_lshlrev_b32 v24, 16, v136\n"
      "v_and_b32 v25, 0xffff0000, v136\n"
      "v_lshlrev_b32 v26, 16, v137\n"
      "v_and_b32 v27, 0xffff0000, v137\n"
      "v_lshlrev_b32 v28, 16, v138\n"
      "v_and_b32 v29, 0xffff0000, v138\n"
      "v_lshlrev_b32 v30, 16, v140\n"
      "v_and_b32 v31, 0xffff0000, v140\n"
      "v_lshlrev_b32 v50, 16, v141\n"
      "v_and_b32 v51, 0xffff0000, v141\n"
      "v_pk_mul_f32 v[96:97], v[96:97], v[6:7] op_sel_hi:[1,0]\n"
      "v_pk_mul_f32 v[98:99], v[98:99], v[6:7] op_sel_hi:[1,0]\n"
      "v_pk_mul_f32 v[100:101], v[100:101], v[6:7] op_sel_hi:[1,0]\n"
      "v_pk_mul_f32 v[102:103], v[102:103], v[6:7] op_sel_hi:[1,0]\n"
      "v_pk_fma_f32 v[96:97], v[34:35], v[8:9], v[96:97] op_sel_hi:[1,0,1]\n"
      "v_pk_fma_f32 v[98:99], v[36:37], v[8:9], v[98:99] op_sel_hi:[1,0,1]\n"
      "v_pk_fma_f32 v[100:101], v[38:39], v[8:9], v[100:101] op_sel_hi:[1,0,1]\n"
      "v_pk_fma_f32 v[102:103], v[40:41], v[8:9], v[102:103] op_sel_hi:[1,0,1]\n"
      "v_cvt_pk_bf16_f32 v96, v96, v97\n"
      "v_cvt_pk_bf16_f32 v97, v98, v99\n"
      "v_cvt_pk_bf16_f32 v98, v100, v101\n"
      "v_cvt_pk_bf16_f32 v99, v102, v103\n"
      "buffer_store_dwordx4 v[96:99], v1, s[40:43], s59 offen offset:2048 sc1\n"
      "v_pk_mul_f32 v[104:105], v[104:105], v[6:7] op_sel_hi:[1,0]\n"
      "v_pk_mul_f32 v[106:107], v[106:107], v[6:7] op_sel_hi:[1,0]\n"
      "v_pk_mul_f32 v[108:109], v[108:109], v[6:7] op_sel_hi:[1,0]\n"
      "v_pk_mul_f32 v[110:111], v[110:111], v[6:7] op_sel_hi:[1,0]\n"
      "v_pk_fma_f32 v[104:105], v[42:43], v[8:9], v[104:105] op_sel_hi:[1,0,1]\n"
      "v_pk_fma_f32 v[106:107], v[44:45], v[8:9], v[106:107] op_sel_hi:[1,0,1]\n"
      "v_pk_fma_f32 v[108:109], v[46:47], v[8:9], v[108:109] op_sel_hi:[1,0,1]\n"
      "v_pk_fma_f32 v[110:111], v[16:17], v[8:9], v[110:111] op_sel_hi:[1,0,1]\n"
      "v_cvt_pk_bf16_f32 v104, v104, v105\n"
      "v_cvt_pk_bf16_f32 v105, v106, v107\n"
      "v_cvt_pk_bf16_f32 v106, v108, v109\n"
      "v_cvt_pk_bf16_f32 v107, v110, v111\n"
      "buffer_store_dwordx4 v[104:107], v1, s[40:43], s59 offen offset:2560 sc1\n"
      "v_pk_mul_f32 v[112:113], v[112:113], v[6:7] op_sel_hi:[1,0]\n"
      "v_pk_mul_f32 v[114:115], v[114:115], v[6:7] op_sel_hi:[1,0]\n"
      "v_pk_mul_f32 v[116:117], v[116:117], v[6:7] op_sel_hi:[1,0]\n"
      "v_pk_mul_f32 v[118:119], v[118:119], v[6:7] op_sel_hi:[1,0]\n"
      "v_pk_fma_f32 v[112:113], v[18:19], v[8:9], v[112:113] op_sel_hi:[1,0,1]\n"
      "v_pk_fma_f32 v[114:115], v[20:21], v[8:9], v[114:115] op_sel_hi:[1,0,1]\n"
      "v_pk_fma_f32 v[116:117], v[22:23], v[8:9], v[116:117] op_sel_hi:[1,0,1]\n"
      "v_pk_fma_f32 v[118:119], v[24:25], v[8:9], v[118:119] op_sel_hi:[1,0,1]\n"
      "v_cvt_pk_bf16_f32 v112, v112, v113\n"
      "v_cvt_pk_bf16_f32 v113, v114, v115\n"
      "v_cvt_pk_bf16_f32 v114, v116, v117\n"
      "v_cvt_pk_bf16_f32 v115, v118, v119\n"
      "buffer_store_dwordx4 v[112:115], v1, s[40:43], s59 offen offset:3072 sc1\n"
      "v_pk_mul_f32 v[120:121], v[120:121], v[6:7] op_sel_hi:[1,0]\n"
      "v_pk_mul_f32 v[122:123], v[122:123], v[6:7] op_sel_hi:[1,0]\n"
      "v_pk_mul_f32 v[124:125], v[124:125], v[6:7] op_sel_hi:[1,0]\n"
      "v_pk_mul_f32 v[126:127], v[126:127], v[6:7] op_sel_hi:[1,0]\n"
      "v_pk_fma_f32 v[120:121], v[26:27], v[8:9], v[120:121] op_sel_hi:[1,0,1]\n"
      "v_pk_fma_f32 v[122:123], v[28:29], v[8:9], v[122:123] op_sel_hi:[1,0,1]\n"
      "v_pk_fma_f32 v[124:125], v[30:31], v[8:9], v[124:125] op_sel_hi:[1,0,1]\n"
      "v_pk_fma_f32 v[126:127], v[50:51], v[8:9], v[126:127] op_sel_hi:[1,0,1]\n"
      "v_cvt_pk_bf16_f32 v120, v120, v121\n"
      "v_cvt_pk_bf16_f32 v121, v122, v123\n"
      "v_cvt_pk_bf16_f32 v122, v124, v125\n"
      "v_cvt_pk_bf16_f32 v123, v126, v127\n"
      "buffer_store_dwordx4 v[120:123], v1, s[40:43], s59 offen offset:3584 sc1\n"
      "s_branch .Lgq_epi_done\n"
      ".Lgq_e_epi:\n"
      "s_waitcnt lgkmcnt(8)\n"
      "v_mfma_f32_32x32x16_bf16 v[64:79], a[64:67], v[144:147], v[64:79]\n"
      "v_add_f32 v128, v128, v32\n"
      "v_add_f32 v129, v129, v33\n"
      "v_add_f32 v128, v128, v34\n"
      "v_add_f32 v129, v129, v35\n"
      "v_add_f32 v128, v128, v36\n"
      "v_add_f32 v129, v129, v37\n"
      "v_mfma_f32_32x32x16_bf16 v[80:95], a[68:71], v[144:147], v[80:95]\n"
      "v_add_f32 v128, v128, v38\n"
      "v_add_f32 v129, v129, v39\n"
      "v_add_f32 v128, v128, v40\n"
      "v_add_f32 v129, v129, v41\n"
      "v_add_f32 v128, v128, v42\n"
      "v_add_f32 v129, v129, v43\n"
      "s_waitcnt lgkmcnt(0)\n"
      "v_mfma_f32_32x32x16_bf16 v[64:79], a[80:83], v[148:151], v[64:79]\n"
      "v_add_f32 v128, v128, v44\n"
      "v_add_f32 v129, v129, v45\n"
      "v_add_f32 v128, v128, v46\n"
      "v_add_f32 v129, v129, v47\n"
      "v_mfma_f32_32x32x16_bf16 v[80:95], a[84:87], v[148:151], v[80:95]\n"
      "v_add_f32 v6, v128, v129\n"
      "v_mov_b32 v7, v6\n"
      "s_nop 0\n"
      "s_nop 0\n"
      "v_permlane32_swap_b32 v6, v7\n"
      "v_add_f32 v7, v6, v7\n"
      "v_mfma_f32_32x32x16_bf16 v[96:111], a[72:75], v[144:147], v[96:111]\n"
      "v_rcp_f32 v6, v7\n"
      "v_log_f32 v7, v7\n"
      "s_nop 0\n"
      "v_sub_f32 v7, v7, v48\n"
      "v_mfma_f32_32x32x16_bf16 v[96:111], a[88:91], v[148:151], v[96:111]\n"
      "v_mfma_f32_32x32x16_bf16 v[112:127], a[76:79], v[144:147], v[112:127]\n"
      "v_mfma_f32_32x32x16_bf16 v[112:127], a[92:95], v[148:151], v[112:127]\n"
      "v_mov_b32 v142, v7\n"
      "v_pk_mul_f32 v[64:65], v[64:65], v[6:7] op_sel_hi:[1,0]\n"
      "v_pk_mul_f32 v[66:67], v[66:67], v[6:7] op_sel_hi:[1,0]\n"
      "v_pk_mul_f32 v[68:69], v[68:69], v[6:7] op_sel_hi:[1,0]\n"
      "v_pk_mul_f32 v[70:71], v[70:71], v[6:7] op_sel_hi:[1,0]\n"
      "v_cvt_pk_bf16_f32 v176, v64, v65\n"
      "v_cvt_pk_bf16_f32 v177, v66, v67\n"
      "v_cvt_pk_bf16_f32 v178, v68, v69\n"
      "v_cvt_pk_bf16_f32 v180, v70, v71\n"
      "buffer_store_dwordx4 v[176:179], v170, s[40:43], s56 offen offset:0 sc1\n"
      "v_pk_mul_f32 v[72:73], v[72:73], v[6:7] op_sel_hi:[1,0]\n"
      "v_pk_mul_f32 v[74:75], v[74:75], v[6:7] op_sel_hi:[1,0]\n"
      "v_pk_mul_f32 v[76:77], v[76:77], v[6:7] op_sel_hi:[1,0]\n"
      "v_pk_mul_f32 v[78:79], v[78:79], v[6:7] op_sel_hi:[1,0]\n"
      "v_cvt_pk_bf16_f32 v181, v72, v73\n"
      "v_cvt_pk_bf16_f32 v182, v74, v75\n"
      "v_cvt_pk_bf16_f32 v184, v76, v77\n"
      "v_cvt_pk_bf16_f32 v185, v78, v79\n"
      "buffer_store_dwordx4 v[180:183], v170, s[40:43], s56 offen offset:1024 sc1\n"
      "v_pk_mul_f32 v[80:81], v[80:81], v[6:7] op_sel_hi:[1,0]\n"
      "v_pk_mul_f32 v[82:83], v[82:83], v[6:7] op_sel_hi:[1,0]\n"
      "v_pk_mul_f32 v[84:85], v[84:85], v[6:7] op_sel_hi:[1,0]\n"
      "v_pk_mul_f32 v[86:87], v[86:87], v[6:7] op_sel_hi:[1,0]\n"
      "v_cvt_pk_bf16_f32 v186, v80, v81\n"
      "v_cvt_pk_bf16_f32 v188, v82, v83\n"
      "v_cvt_pk_bf16_f32 v189, v84, v85\n"
      "v_cvt_pk_bf16_f32 v190, v86, v87\n"
      "buffer_store_dwordx4 v[184:187], v170, s[40:43], s56 offen offset:2048 sc1\n"
      "buffer_store_dwordx4 v[188:191], v170, s[40:43], s56 offen offset:3072 sc1\n"
      "v_pk_mul_f32 v[88:89], v[88:89], v[6:7] op_sel_hi:[1,0]\n"
      "v_pk_mul_f32 v[90:91], v[90:91], v[6:7] op_sel_hi:[1,0]\n"
      "v_pk_mul_f32 v[92:93], v[92:93], v[6:7] op_sel_hi:[1,0]\n"
      "v_pk_mul_f32 v[94:95], v[94:95], v[6:7] op_sel_hi:[1,0]\n"
      "v_cvt_pk_bf16_f32 v192, v88, v89\n"
      "v_cvt_pk_bf16_f32 v193, v90, v91\n"
      "v_cvt_pk_bf16_f32 v194, v92, v93\n"
      "v_cvt_pk_bf16_f32 v196, v94, v95\n"
      "buffer_store_dwordx4 v[192:195], v170, s[40:43], s80 offen offset:0 sc1\n"
      "v_pk_mul_f32 v[96:97], v[96:97], v[6:7] op_sel_hi:[1,0]\n"
      "v_pk_mul_f32 v[98:99], v[98:99], v[6:7] op_sel_hi:[1,0]\n"
      "v_pk_mul_f32 v[100:101], v[100:101], v[6:7] op_sel_hi:[1,0]\n"
      "v_pk_mul_f32 v[102:103], v[102:103], v[6:7] op_sel_hi:[1,0]\n"
      "v_cvt_pk_bf16_f32 v197, v96, v97\n"
      "v_cvt_pk_bf16_f32 v198, v98, v99\n"
      "v_cvt_pk_bf16_f32 v200, v100, v101\n"
      "v_cvt_pk_bf16_f32 v201, v102, v103\n"
      "buffer_store_dwordx4 v[196:199], v170, s[40:43], s80 offen offset:1024 sc1\n"
      "v_pk_mul_f32 v[104:105], v[104:105], v[6:7] op_sel_hi:[1,0]\n"
      "v_pk_mul_f32 v[106:107], v[106:107], v[6:7] op_sel_hi:[1,0]\n"
      "v_pk_mul_f32 v[108:109], v[108:109], v[6:7] op_sel_hi:[1,0]\n"
      "v_pk_mul_f32 v[110:111], v[110:111], v[6:7] op_sel_hi:[1,0]\n"
      "v_cvt_pk_bf16_f32 v202, v104, v105\n"
      "v_cvt_pk_bf16_f32 v204, v106, v107\n"
      "v_cvt_pk_bf16_f32 v205, v108, v109\n"
      "v_cvt_pk_bf16_f32 v206, v110, v111\n"
      "buffer_store_dwordx4 v[200:203], v170, s[40:43], s80 offen offset:2048 sc1\n"
      "buffer_store_dwordx4 v[204:207], v170, s[40:43], s80 offen offset:3072 sc1\n"
      "v_pk_mul_f32 v[112:113], v[112:113], v[6:7] op_sel_hi:[1,0]\n"
      "v_pk_mul_f32 v[114:115], v[114:115], v[6:7] op_sel_hi:[1,0]\n"
      "v_pk_mul_f32 v[116:117], v[116:117], v[6:7] op_sel_hi:[1,0]\n"
      "v_pk_mul_f32 v[118:119], v[118:119], v[6:7] op_sel_hi:[1,0]\n"
      "v_cvt_pk_bf16_f32 v132, v112, v113\n"
      "v_cvt_pk_bf16_f32 v133, v114, v115\n"
      "v_cvt_pk_bf16_f32 v134, v116, v117\n"
      "v_cvt_pk_bf16_f32 v136, v118, v119\n"
      "buffer_store_dwordx4 v[132:135], v170, s[40:43], s81 offen offset:0 sc1\n"
      "v_pk_mul_f32 v[120:121], v[120:121], v[6:7] op_sel_hi:[1,0]\n"
      "v_pk_mul_f32 v[122:123], v[122:123], v[6:7] op_sel_hi:[1,0]\n"
      "v_pk_mul_f32 v[124:125], v[124:125], v[6:7] op_sel_hi:[1,0]\n"
      "v_pk_mul_f32 v[126:127], v[126:127], v[6:7] op_sel_hi:[1,0]\n"
      "v_cvt_pk_bf16_f32 v137, v120, v121\n"
      "v_cvt_pk_bf16_f32 v138, v122, v123\n"
      "v_cvt_pk_bf16_f32 v140, v124, v125\n"
      "v_cvt_pk_bf16_f32 v141, v126, v127\n"
      "buffer_store_dwordx4 v[136:139], v170, s[40:43], s81 offen offset:1024 sc1\n"
      "buffer_store_dwordx4 v[140:143], v170, s[40:43], s81 offen offset:2048 sc1\n"
      ".Lgq_epi_done:\n"
      "s_or_b32 s85, s18, s19\n"
      "v_lshrrev_b32 v8, 3, v172\n"
      "v_lshl_add_u32 v8, v8, 7, v171\n"
      "s_mov_b32 s81, 4096\n"
      "s_cmp_lg_u32 s85, 0\n"
      "s_cbranch_scc1 .Lgq_e_skip0\n"
      "buffer_load_dwordx2 v[48:49], v8, s[40:43], s81 offen offset:0 sc1\n"
      "buffer_load_dwordx2 v[50:51], v8, s[40:43], s81 offen offset:1024 sc1\n"
      "buffer_load_dwordx2 v[52:53], v8, s[40:43], s81 offen offset:2048 sc1\n"
      "buffer_load_dwordx2 v[54:55], v8, s[40:43], s81 offen offset:3072 sc1\n"
      ".Lgq_e_skip0:\n"
      "v_add_u32 v14, 0x10000, v170\n"
      "v_add_u32 v9, 0x20000, v170\n"
      "v_subrev_u32 v156, s77, v156\n"
      "v_subrev_u32 v157, s77, v157\n"
      "v_add_u32 v154, 65536, v156\n"
      "v_add_u32 v155, 65536, v157\n"
      "v_and_b32 v10, 3, v172\n"
      "v_lshlrev_b32 v10, 7, v10\n"
      "s_waitcnt vmcnt(0) lgkmcnt(0)\n"
      "s_barrier\n"
      "s_or_b32 s80, s79, s19\n"
      "s_cmp_lg_u32 s80, 0\n"
      "s_cbranch_scc1 .Lgq_of_nofl\n"
      "s_mov_b64 exec, 1\n"
      "buffer_store_dwordx2 v[4:5], off, s[40:43], s60 sc1\n"
      "s_mov_b64 exec, -1\n"
      ".Lgq_of_nofl:\n"
      "ds_read_b64_tr_b16 v[16:17], v154 offset:0\n"
      "ds_read_b64_tr_b16 v[18:19], v155 offset:0\n"
      "ds_read_b64_tr_b16 v[20:21], v154 offset:8\n"
      "ds_read_b64_tr_b16 v[22:23], v155 offset:8\n"
      "ds_read_b64_tr_b16 v[24:25], v154 offset:8192\n"
      "ds_read_b64_tr_b16 v[26:27], v155 offset:8192\n"
      "ds_read_b64_tr_b16 v[28:29], v154 offset:8200\n"
      "ds_read_b64_tr_b16 v[30:31], v155 offset:8200\n"
      "ds_read_b64_tr_b16 v[32:33], v154 offset:49152\n"
      "ds_read_b64_tr_b16 v[34:35], v155 offset:49152\n"
      "ds_read_b64_tr_b16 v[36:37], v154 offset:49160\n"
      "ds_read_b64_tr_b16 v[38:39], v155 offset:49160\n"
      "ds_read_b64_tr_b16 v[40:41], v154 offset:57344\n"
      "ds_read_b64_tr_b16 v[42:43], v155 offset:57344\n"
      "ds_read_b64_tr_b16 v[44:45], v154 offset:57352\n"
      "ds_read_b64_tr_b16 v[46:47], v155 offset:57352\n"
      "s_cmp_lg_u32 s85, 0\n"
      "s_cbranch_scc1 .Lgq_e_skip1\n"
      "s_mov_b32 s87, 65536\n"
      ".Lgq_e_chk:\n"
      "s_mov_b64 s[4:5], exec\n"
      "v_cmp_eq_u64 vcc, s[48:49], v[48:49]\n"
      "s_nop 1\n"
      "s_and_b64 s[4:5], s[4:5], vcc\n"
      "v_cmp_eq_u64 vcc, s[48:49], v[50:51]\n"
      "s_nop 1\n"
      "s_and_b64 s[4:5], s[4:5], vcc\n"
      "v_cmp_eq_u64 vcc, s[48:49], v[52:53]\n"
      "s_nop 1\n"
      "s_and_b64 s[4:5], s[4:5], vcc\n"
      "v_cmp_eq_u64 vcc, s[48:49], v[54:55]\n"
      "s_nop 1\n"
      "s_and_b64 s[4:5], s[4:5], vcc\n"
      "s_cmp_eq_u64 s[4:5], exec\n"
      "s_cbranch_scc1 .Lgq_e_store\n"
      "buffer_load_dwordx2 v[48:49], v8, s[40:43], s81 offen offset:0 sc1\n"
      "buffer_load_dwordx2 v[50:51], v8, s[40:43], s81 offen offset:1024 sc1\n"
      "buffer_load_dwordx2 v[52:53], v8, s[40:43], s81 offen offset:2048 sc1\n"
      "buffer_load_dwordx2 v[54:55], v8, s[40:43], s81 offen offset:3072 sc1\n"
      "s_waitcnt vmcnt(0)\n"
      "s_sub_u32 s87, s87, 1\n"
      "s_cmp_lg_u32 s87, 0\n"
      "s_cbranch_scc1 .Lgq_e_chk\n"
      ".Lgq_e_store:\n"
      "s_mov_b64 exec, 1\n"
      "buffer_store_dwordx2 v[4:5], off, s[40:43], 0 sc1\n"
      "s_mov_b64 exec, -1\n"
      ".Lgq_e_skip1:\n"
      "v_mov_b32 v131, s48\n"
      "v_mov_b32 v135, s48\n"
      "v_mov_b32 v139, s48\n"
      "v_mov_b32 v143, s48\n"
      "v_mov_b32 v147, s48\n"
      "v_mov_b32 v151, s48\n"
      "v_and_b32 v3, 15, v172\n"
      "v_lshrrev_b32 v6, 4, v172\n"
      "v_lshlrev_b32 v15, 11, v3\n"
      "v_lshl_add_u32 v15, v6, 4, v15\n"
      "s_lshl_b32 s83, s70, 13\n"
      "s_add_u32 s83, s83, s63\n"
      "s_lshl_b32 s84, s70, 15\n"
      "s_add_u32 s84, s84, s63\n"
      "s_add_u32 s81, s70, 4\n"
      "s_and_b32 s81, s81, 3\n"
      "s_lshl_b32 s82, s81, 15\n"
      "s_add_u32 s58, s62, s82\n"
      "s_add_u32 s81, s70, 3\n"
      "s_and_b32 s81, s81, 3\n"
      "s_lshl_b32 s82, s81, 15\n"
      "s_add_u32 s57, s62, s82\n"
      "s_add_u32 s59, s83, s82\n"
      "s_lshl_b32 s82, s81, 13\n"
      "s_add_u32 s75, s84, s82\n"
      "s_mov_b32 s91, 0x7060503\n"
      "s_add_u32 s81, s70, 2\n"
      "s_and_b32 s81, s81, 3\n"
      "s_lshl_b32 s82, s81, 15\n"
      "s_add_u32 s56, s62, s82\n"
      "s_add_u32 s95, s83, s82\n"
      "s_lshl_b32 s82, s81, 13\n"
      "s_add_u32 s74, s84, s82\n"
      "s_mov_b32 s90, 0x7060502\n"
      "s_add_u32 s81, s70, 1\n"
      "s_and_b32 s81, s81, 3\n"
      "s_lshl_b32 s82, s81, 15\n"
      "s_add_u32 s62, s62, s82\n"
      "s_add_u32 s94, s83, s82\n"
      "s_lshl_b32 s82, s81, 13\n"
      "s_add_u32 s73, s84, s82\n"
      "s_mov_b32 s89, 0x7060501\n"
      "s_mov_b32 s92, 0x0c04000c\n"
      "s_cmp_lg_u32 s19, 0\n"
      "s_cbranch_scc1 .Lgq_o_ok\n"
      "s_mov_b32 s87, 65536\n"
      ".Lgq_o_spin:\n"
      "buffer_load_dwordx2 v[2:3], v10, s[40:43], s61 offen sc1\n"
      "s_waitcnt vmcnt(0)\n"
      "v_cmp_eq_u64 vcc, s[48:49], v[2:3]\n"
      "s_nop 1\n"
      "s_cmp_eq_u64 vcc, exec\n"
      "s_cbranch_scc1 .Lgq_o_ok\n"
      "s_sleep 1\n"
      "s_sub_u32 s87, s87, 1\n"
      "s_cmp_lg_u32 s87, 0\n"
      "s_cbranch_scc1 .Lgq_o_spin\n"
      ".Lgq_o_ok:\n"
      "s_barrier\n"
      "s_add_u32 s80, s62, 0\n"
      "s_add_u32 m0, s71, 0\n"
      "s_nop 0\n"
      "buffer_load_dwordx4 v170, s[40:43], s80 offen offset:0 sc1 lds\n"
      "buffer_load_dwordx4 v170, s[40:43], s80 offen offset:1024 sc1 lds\n"
      "buffer_load_dwordx4 v170, s[40:43], s80 offen offset:2048 sc1 lds\n"
      "buffer_load_dwordx4 v170, s[40:43], s80 offen offset:3072 sc1 lds\n"
      "s_add_u32 s80, s62, 16384\n"
      "s_add_u32 m0, s71, 16384\n"
      "s_nop 0\n"
      "buffer_load_dwordx4 v170, s[40:43], s80 offen offset:0 sc1 lds\n"
      "buffer_load_dwordx4 v170, s[40:43], s80 offen offset:1024 sc1 lds\n"
      "buffer_load_dwordx4 v170, s[40:43], s80 offen offset:2048 sc1 lds\n"
      "buffer_load_dwordx4 v170, s[40:43], s80 offen offset:3072 sc1 lds\n"
      "s_add_u32 s80, s56, 0\n"
      "s_add_u32 m0, s71, 32768\n"
      "s_nop 0\n"
      "buffer_load_dwordx4 v170, s[40:43], s80 offen offset:0 sc1 lds\n"
      "buffer_load_dwordx4 v170, s[40:43], s80 offen offset:1024 sc1 lds\n"
      "buffer_load_dwordx4 v170, s[40:43], s80 offen offset:2048 sc1 lds\n"
      "buffer_load_dwordx4 v170, s[40:43], s80 offen offset:3072 sc1 lds\n"
      "s_waitcnt vmcnt(8)\n"
      "s_barrier\n"
      "ds_read_b128 a[64:67], v170 offset:0\n"
      "ds_read_b128 a[68:71], v170 offset:1024\n"
      "ds_read_b128 a[72:75], v170 offset:2048\n"
      "ds_read_b128 a[76:79], v170 offset:3072\n"
      "ds_read_b128 a[80:83], v170 offset:4096\n"
      "ds_read_b128 a[84:87], v170 offset:5120\n"
      "ds_read_b128 a[88:91], v170 offset:6144\n"
      "ds_read_b128 a[92:95], v170 offset:7168\n"
      "s_waitcnt lgkmcnt(4)\n"
      "v_mfma_f32_16x16x32_bf16 v[176:179], a[112:115], a[64:67], 0\n"
      "ds_read_b128 a[96:99], v170 offset:8192\n"
      "v_mfma_f32_16x16x32_bf16 v[180:183], a[116:119], a[64:67], 0\n"
      "v_mfma_f32_16x16x32_bf16 v[176:179], a[120:123], a[68:71], v[176:179]\n"
      "ds_read_b128 a[100:103], v170 offset:9216\n"
      "v_mfma_f32_16x16x32_bf16 v[180:183], a[124:127], a[68:71], v[180:183]\n"
      "s_add_u32 s80, s56, 16384\n"
      "s_add_u32 m0, s71, 49152\n"
      "v_mfma_f32_16x16x32_bf16 v[176:179], a[128:131], a[72:75], v[176:179]\n"
      "ds_read_b128 a[64:67], v170 offset:10240\n"
      "v_mfma_f32_16x16x32_bf16 v[180:183], a[132:135], a[72:75], v[180:183]\n"
      "buffer_load_dwordx4 v170, s[40:43], s80 offen offset:0 sc1 lds\n"
      "v_mfma_f32_16x16x32_bf16 v[176:179], a[136:139], a[76:79], v[176:179]\n"
      "ds_read_b128 a[68:71], v170 offset:11264\n"
      "v_mfma_f32_16x16x32_bf16 v[180:183], a[140:143], a[76:79], v[180:183]\n"
      "s_waitcnt lgkmcnt(4)\n"
      "v_mfma_f32_16x16x32_bf16 v[176:179], a[144:147], a[80:83], v[176:179]\n"
      "ds_read_b128 a[72:75], v170 offset:12288\n"
      "v_mfma_f32_16x16x32_bf16 v[180:183], a[148:151], a[80:83], v[180:183]\n"
      "buffer_load_dwordx4 v170, s[40:43], s80 offen offset:1024 sc1 lds\n"
      "v_mfma_f32_16x16x32_bf16 v[176:179], a[152:155], a[84:87], v[176:179]\n"
      "ds_read_b128 a[76:79], v170 offset:13312\n"
      "v_mfma_f32_16x16x32_bf16 v[180:183], a[156:159], a[84:87], v[180:183]\n"
      "v_mfma_f32_16x16x32_bf16 v[176:179], a[160:163], a[88:91], v[176:179]\n"
      "ds_read_b128 a[80:83], v170 offset:14336\n"
      "v_mfma_f32_16x16x32_bf16 v[180:183], a[164:167], a[88:91], v[180:183]\n"
      "buffer_load_dwordx4 v170, s[40:43], s80 offen offset:2048 sc1 lds\n"
      "v_mfma_f32_16x16x32_bf16 v[176:179], a[168:171], a[92:95], v[176:179]\n"
      "ds_read_b128 a[84:87], v170 offset:15360\n"
      "v_mfma_f32_16x16x32_bf16 v[180:183], a[172:175], a[92:95], v[180:183]\n"
      "s_waitcnt lgkmcnt(4)\n"
      "v_mfma_f32_16x16x32_bf16 v[176:179], a[176:179], a[96:99], v[176:179]\n"
      "s_waitcnt vmcnt(7)\n"
      "s_barrier\n"
      "ds_read_b128 a[88:91], v170 offset:16384\n"
      "v_mfma_f32_16x16x32_bf16 v[180:183], a[180:183], a[96:99], v[180:183]\n"
      "buffer_load_dwordx4 v170, s[40:43], s80 offen offset:3072 sc1 lds\n"
      "v_mfma_f32_16x16x32_bf16 v[176:179], a[184:187], a[100:103], v[176:179]\n"
      "ds_read_b128 a[92:95], v170 offset:17408\n"
      "v_mfma_f32_16x16x32_bf16 v[180:183], a[188:191], a[100:103], v[180:183]\n"
      "v_mfma_f32_16x16x32_bf16 v[176:179], a[192:195], a[64:67], v[176:179]\n"
      "ds_read_b128 a[96:99], v170 offset:18432\n"
      "v_mfma_f32_16x16x32_bf16 v[180:183], a[196:199], a[64:67], v[180:183]\n"
      "v_mfma_f32_16x16x32_bf16 v[176:179], a[200:203], a[68:71], v[176:179]\n"
      "ds_read_b128 a[100:103], v170 offset:19456\n"
      "v_mfma_f32_16x16x32_bf16 v[180:183], a[204:207], a[68:71], v[180:183]\n"
      "s_waitcnt lgkmcnt(4)\n"
      "v_mfma_f32_16x16x32_bf16 v[176:179], a[208:211], a[72:75], v[176:179]\n"
      "ds_read_b128 a[64:67], v170 offset:20480\n"
      "v_mfma_f32_16x16x32_bf16 v[180:183], a[212:215], a[72:75], v[180:183]\n"
      "v_mfma_f32_16x16x32_bf16 v[176:179], a[216:219], a[76:79], v[176:179]\n"
      "ds_read_b128 a[68:71], v170 offset:21504\n"
      "v_mfma_f32_16x16x32_bf16 v[180:183], a[220:223], a[76:79], v[180:183]\n"
      "v_mfma_f32_16x16x32_bf16 v[176:179], a[224:227], a[80:83], v[176:179]\n"
      "ds_read_b128 a[72:75], v170 offset:22528\n"
      "v_mfma_f32_16x16x32_bf16 v[180:183], a[228:231], a[80:83], v[180:183]\n"
      "v_mfma_f32_16x16x32_bf16 v[176:179], a[232:235], a[84:87], v[176:179]\n"
      "ds_read_b128 a[76:79], v170 offset:23552\n"
      "v_mfma_f32_16x16x32_bf16 v[180:183], a[236:239], a[84:87], v[180:183]\n"
      "s_waitcnt lgkmcnt(4)\n"
      "v_mfma_f32_16x16x32_bf16 v[176:179], a[240:243], a[88:91], v[176:179]\n"
      "ds_read_b128 a[80:83], v170 offset:24576\n"
      "v_mfma_f32_16x16x32_bf16 v[180:183], a[244:247], a[88:91], v[180:183]\n"
      "v_mfma_f32_16x16x32_bf16 v[176:179], a[248:251], a[92:95], v[176:179]\n"
      "ds_read_b128 a[84:87], v170 offset:25600\n"
      "v_mfma_f32_16x16x32_bf16 v[180:183], a[252:255], a[92:95], v[180:183]\n"
      "s_add_u32 s80, s57, 0\n"
      "s_add_u32 m0, s71, 81920\n"
      "v_mfma_f32_16x16x32_bf16 v[176:179], a[0:3], a[96:99], v[176:179]\n"
      "ds_read_b128 a[88:91], v170 offset:26624\n"
      "v_mfma_f32_16x16x32_bf16 v[180:183], a[4:7], a[96:99], v[180:183]\n"
      "buffer_load_dwordx4 v170, s[40:43], s80 offen offset:0 sc1 lds\n"
      "v_mfma_f32_16x16x32_bf16 v[176:179], a[8:11], a[100:103], v[176:179]\n"
      "ds_read_b128 a[92:95], v170 offset:27648\n"
      "v_mfma_f32_16x16x32_bf16 v[180:183], a[12:15], a[100:103], v[180:183]\n"
      "s_waitcnt lgkmcnt(4)\n"
      "v_mfma_f32_16x16x32_bf16 v[176:179], a[16:19], a[64:67], v[176:179]\n"
      "ds_read_b128 a[96:99], v170 offset:28672\n"
      "v_mfma_f32_16x16x32_bf16 v[180:183], a[20:23], a[64:67], v[180:183]\n"
      "buffer_load_dwordx4 v170, s[40:43], s80 offen offset:1024 sc1 lds\n"
      "v_mfma_f32_16x16x32_bf16 v[176:179], a[24:27], a[68:71], v[176:179]\n"
      "ds_read_b128 a[100:103], v170 offset:29696\n"
      "v_mfma_f32_16x16x32_bf16 v[180:183], a[28:31], a[68:71], v[180:183]\n"
      "v_mfma_f32_16x16x32_bf16 v[176:179], v[208:211], a[72:75], v[176:179]\n"
      "ds_read_b128 a[64:67], v170 offset:30720\n"
      "v_mfma_f32_16x16x32_bf16 v[180:183], v[212:215], a[72:75], v[180:183]\n"
      "buffer_load_dwordx4 v170, s[40:43], s80 offen offset:2048 sc1 lds\n"
      "v_mfma_f32_16x16x32_bf16 v[176:179], v[216:219], a[76:79], v[176:179]\n"
      "ds_read_b128 a[68:71], v170 offset:31744\n"
      "v_mfma_f32_16x16x32_bf16 v[180:183], v[220:223], a[76:79], v[180:183]\n"
      "s_waitcnt lgkmcnt(4)\n"
      "v_mfma_f32_16x16x32_bf16 v[176:179], v[224:227], a[80:83], v[176:179]\n"
      "s_waitcnt vmcnt(7)\n"
      "s_barrier\n"
      "ds_read_b128 a[72:75], v170 offset:32768\n"
      "v_mfma_f32_16x16x32_bf16 v[180:183], v[228:231], a[80:83], v[180:183]\n"
      "buffer_load_dwordx4 v170, s[40:43], s80 offen offset:3072 sc1 lds\n"
      "v_mfma_f32_16x16x32_bf16 v[176:179], v[232:235], a[84:87], v[176:179]\n"
      "ds_read_b128 a[76:79], v170 offset:33792\n"
      "v_mfma_f32_16x16x32_bf16 v[180:183], v[236:239], a[84:87], v[180:183]\n"
      "v_mfma_f32_16x16x32_bf16 v[176:179], v[240:243], a[88:91], v[176:179]\n"
      "ds_read_b128 a[80:83], v170 offset:34816\n"
      "v_mfma_f32_16x16x32_bf16 v[180:183], v[244:247], a[88:91], v[180:183]\n"
      "v_mfma_f32_16x16x32_bf16 v[176:179], v[248:251], a[92:95], v[176:179]\n"
      "ds_read_b128 a[84:87], v170 offset:35840\n"
      "v_mfma_f32_16x16x32_bf16 v[180:183], v[252:255], a[92:95], v[180:183]\n"
      "s_waitcnt lgkmcnt(4)\n"
      "v_mfma_f32_16x16x32_bf16 v[176:179], v[16:19], a[96:99], v[176:179]\n"
      "ds_read_b128 a[88:91], v170 offset:36864\n"
      "v_mfma_f32_16x16x32_bf16 v[180:183], v[20:23], a[96:99], v[180:183]\n"
      "v_mfma_f32_16x16x32_bf16 v[176:179], v[24:27], a[100:103], v[176:179]\n"
      "ds_read_b128 a[92:95], v170 offset:37888\n"
      "v_mfma_f32_16x16x32_bf16 v[180:183], v[28:31], a[100:103], v[180:183]\n"
      "v_mfma_f32_16x16x32_bf16 v[176:179], v[32:35], a[64:67], v[176:179]\n"
      "ds_read_b128 a[96:99], v170 offset:38912\n"
      "v_mfma_f32_16x16x32_bf16 v[180:183], v[36:39], a[64:67], v[180:183]\n"
      "v_mfma_f32_16x16x32_bf16 v[176:179], v[40:43], a[68:71], v[176:179]\n"
      "ds_read_b128 a[100:103], v170 offset:39936\n"
      "v_mfma_f32_16x16x32_bf16 v[180:183], v[44:47], a[68:71], v[180:183]\n"
      "s_waitcnt lgkmcnt(4)\n"
      "v_mfma_f32_16x16x32_bf16 v[184:187], a[112:115], a[72:75], 0\n"
      "ds_read_b128 a[64:67], v170 offset:40960\n"
      "v_mfma_f32_16x16x32_bf16 v[188:191], a[116:119], a[72:75], 0\n"
      "v_mfma_f32_16x16x32_bf16 v[184:187], a[120:123], a[76:79], v[184:187]\n"
      "ds_read_b128 a[68:71], v170 offset:41984\n"
      "v_mfma_f32_16x16x32_bf16 v[188:191], a[124:127], a[76:79], v[188:191]\n"
      "s_add_u32 s80, s57, 16384\n"
      "s_add_u32 m0, s71, 98304\n"
      "v_mfma_f32_16x16x32_bf16 v[184:187], a[128:131], a[80:83], v[184:187]\n"
      "ds_read_b128 a[72:75], v170 offset:43008\n"
      "v_mfma_f32_16x16x32_bf16 v[188:191], a[132:135], a[80:83], v[188:191]\n"
      "buffer_load_dwordx4 v170, s[40:43], s80 offen offset:0 sc1 lds\n"
      "v_mfma_f32_16x16x32_bf16 v[184:187], a[136:139], a[84:87], v[184:187]\n"
      "ds_read_b128 a[76:79], v170 offset:44032\n"
      "v_mfma_f32_16x16x32_bf16 v[188:191], a[140:143], a[84:87], v[188:191]\n"
      "s_waitcnt lgkmcnt(4)\n"
      "v_mfma_f32_16x16x32_bf16 v[184:187], a[144:147], a[88:91], v[184:187]\n"
      "ds_read_b128 a[80:83], v170 offset:45056\n"
      "v_mfma_f32_16x16x32_bf16 v[188:191], a[148:151], a[88:91], v[188:191]\n"
      "buffer_load_dwordx4 v170, s[40:43], s80 offen offset:1024 sc1 lds\n"
      "v_mfma_f32_16x16x32_bf16 v[184:187], a[152:155], a[92:95], v[184:187]\n"
      "ds_read_b128 a[84:87], v170 offset:46080\n"
      "v_mfma_f32_16x16x32_bf16 v[188:191], a[156:159], a[92:95], v[188:191]\n"
      "v_mfma_f32_16x16x32_bf16 v[184:187], a[160:163], a[96:99], v[184:187]\n"
      "ds_read_b128 a[88:91], v170 offset:47104\n"
      "v_mfma_f32_16x16x32_bf16 v[188:191], a[164:167], a[96:99], v[188:191]\n"
      "buffer_load_dwordx4 v170, s[40:43], s80 offen offset:2048 sc1 lds\n"
      "v_mfma_f32_16x16x32_bf16 v[184:187], a[168:171], a[100:103], v[184:187]\n"
      "ds_read_b128 a[92:95], v170 offset:48128\n"
      "v_mfma_f32_16x16x32_bf16 v[188:191], a[172:175], a[100:103], v[188:191]\n"
      "s_waitcnt lgkmcnt(4)\n"
      "v_mfma_f32_16x16x32_bf16 v[184:187], a[176:179], a[64:67], v[184:187]\n"
      "s_waitcnt vmcnt(7)\n"
      "s_barrier\n"
      "ds_read_b128 a[96:99], v170 offset:49152\n"
      "v_mfma_f32_16x16x32_bf16 v[188:191], a[180:183], a[64:67], v[188:191]\n"
      "buffer_load_dwordx4 v170, s[40:43], s80 offen offset:3072 sc1 lds\n"
      "v_mfma_f32_16x16x32_bf16 v[184:187], a[184:187], a[68:71], v[184:187]\n"
      "ds_read_b128 a[100:103], v170 offset:50176\n"
      "v_mfma_f32_16x16x32_bf16 v[188:191], a[188:191], a[68:71], v[188:191]\n"
      "v_mfma_f32_16x16x32_bf16 v[184:187], a[192:195], a[72:75], v[184:187]\n"
      "ds_read_b128 a[64:67], v170 offset:51200\n"
      "v_mfma_f32_16x16x32_bf16 v[188:191], a[196:199], a[72:75], v[188:191]\n"
      "v_mfma_f32_16x16x32_bf16 v[184:187], a[200:203], a[76:79], v[184:187]\n"
      "ds_read_b128 a[68:71], v170 offset:52224\n"
      "v_mfma_f32_16x16x32_bf16 v[188:191], a[204:207], a[76:79], v[188:191]\n"
      "s_waitcnt lgkmcnt(4)\n"
      "v_mfma_f32_16x16x32_bf16 v[184:187], a[208:211], a[80:83], v[184:187]\n"
      "ds_read_b128 a[72:75], v170 offset:53248\n"
      "v_mfma_f32_16x16x32_bf16 v[188:191], a[212:215], a[80:83], v[188:191]\n"
      "v_mfma_f32_16x16x32_bf16 v[184:187], a[216:219], a[84:87], v[184:187]\n"
      "ds_read_b128 a[76:79], v170 offset:54272\n"
      "v_mfma_f32_16x16x32_bf16 v[188:191], a[220:223], a[84:87], v[188:191]\n"
      "v_mfma_f32_16x16x32_bf16 v[184:187], a[224:227], a[88:91], v[184:187]\n"
      "ds_read_b128 a[80:83], v170 offset:55296\n"
      "v_mfma_f32_16x16x32_bf16 v[188:191], a[228:231], a[88:91], v[188:191]\n"
      "v_mfma_f32_16x16x32_bf16 v[184:187], a[232:235], a[92:95], v[184:187]\n"
      "ds_read_b128 a[84:87], v170 offset:56320\n"
      "v_mfma_f32_16x16x32_bf16 v[188:191], a[236:239], a[92:95], v[188:191]\n"
      "s_waitcnt lgkmcnt(4)\n"
      "v_mfma_f32_16x16x32_bf16 v[184:187], a[240:243], a[96:99], v[184:187]\n"
      "ds_read_b128 a[88:91], v170 offset:57344\n"
      "v_mfma_f32_16x16x32_bf16 v[188:191], a[244:247], a[96:99], v[188:191]\n"
      "v_mfma_f32_16x16x32_bf16 v[184:187], a[248:251], a[100:103], v[184:187]\n"
      "ds_read_b128 a[92:95], v170 offset:58368\n"
      "v_mfma_f32_16x16x32_bf16 v[188:191], a[252:255], a[100:103], v[188:191]\n"
      "s_add_u32 s80, s58, 0\n"
      "s_add_u32 m0, s71, 131072\n"
      "v_mfma_f32_16x16x32_bf16 v[184:187], a[0:3], a[64:67], v[184:187]\n"
      "ds_read_b128 a[96:99], v170 offset:59392\n"
      "v_mfma_f32_16x16x32_bf16 v[188:191], a[4:7], a[64:67], v[188:191]\n"
      "buffer_load_dwordx4 v170, s[40:43], s80 offen offset:0 sc1 lds\n"
      "v_mfma_f32_16x16x32_bf16 v[184:187], a[8:11], a[68:71], v[184:187]\n"
      "ds_read_b128 a[100:103], v170 offset:60416\n"
      "v_mfma_f32_16x16x32_bf16 v[188:191], a[12:15], a[68:71], v[188:191]\n"
      "s_waitcnt lgkmcnt(4)\n"
      "v_mfma_f32_16x16x32_bf16 v[184:187], a[16:19], a[72:75], v[184:187]\n"
      "ds_read_b128 a[64:67], v170 offset:61440\n"
      "v_mfma_f32_16x16x32_bf16 v[188:191], a[20:23], a[72:75], v[188:191]\n"
      "buffer_load_dwordx4 v170, s[40:43], s80 offen offset:1024 sc1 lds\n"
      "v_mfma_f32_16x16x32_bf16 v[184:187], a[24:27], a[76:79], v[184:187]\n"
      "ds_read_b128 a[68:71], v170 offset:62464\n"
      "v_mfma_f32_16x16x32_bf16 v[188:191], a[28:31], a[76:79], v[188:191]\n"
      "v_mfma_f32_16x16x32_bf16 v[184:187], v[208:211], a[80:83], v[184:187]\n"
      "ds_read_b128 a[72:75], v170 offset:63488\n"
      "v_mfma_f32_16x16x32_bf16 v[188:191], v[212:215], a[80:83], v[188:191]\n"
      "buffer_load_dwordx4 v170, s[40:43], s80 offen offset:2048 sc1 lds\n"
      "v_mfma_f32_16x16x32_bf16 v[184:187], v[216:219], a[84:87], v[184:187]\n"
      "ds_read_b128 a[76:79], v170 offset:64512\n"
      "v_mfma_f32_16x16x32_bf16 v[188:191], v[220:223], a[84:87], v[188:191]\n"
      "s_waitcnt lgkmcnt(4)\n"
      "v_mfma_f32_16x16x32_bf16 v[184:187], v[224:227], a[88:91], v[184:187]\n"
      "s_waitcnt vmcnt(7)\n"
      "s_barrier\n"
      "ds_read_b128 a[80:83], v14 offset:16384\n"
      "v_mfma_f32_16x16x32_bf16 v[188:191], v[228:231], a[88:91], v[188:191]\n"
      "buffer_load_dwordx4 v170, s[40:43], s80 offen offset:3072 sc1 lds\n"
      "v_mfma_f32_16x16x32_bf16 v[184:187], v[232:235], a[92:95], v[184:187]\n"
      "ds_read_b128 a[84:87], v14 offset:17408\n"
      "v_mfma_f32_16x16x32_bf16 v[188:191], v[236:239], a[92:95], v[188:191]\n"
      "v_mfma_f32_16x16x32_bf16 v[184:187], v[240:243], a[96:99], v[184:187]\n"
      "ds_read_b128 a[88:91], v14 offset:18432\n"
      "v_mfma_f32_16x16x32_bf16 v[188:191], v[244:247], a[96:99], v[188:191]\n"
      "v_mfma_f32_16x16x32_bf16 v[184:187], v[248:251], a[100:103], v[184:187]\n"
      "ds_read_b128 a[92:95], v14 offset:19456\n"
      "v_mfma_f32_16x16x32_bf16 v[188:191], v[252:255], a[100:103], v[188:191]\n"
      "s_waitcnt lgkmcnt(4)\n"
      "v_mfma_f32_16x16x32_bf16 v[184:187], v[16:19], a[64:67], v[184:187]\n"
      "ds_read_b128 a[96:99], v14 offset:20480\n"
      "v_mfma_f32_16x16x32_bf16 v[188:191], v[20:23], a[64:67], v[188:191]\n"
      "v_mfma_f32_16x16x32_bf16 v[184:187], v[24:27], a[68:71], v[184:187]\n"
      "ds_read_b128 a[100:103], v14 offset:21504\n"
      "v_mfma_f32_16x16x32_bf16 v[188:191], v[28:31], a[68:71], v[188:191]\n"
      "v_mfma_f32_16x16x32_bf16 v[184:187], v[32:35], a[72:75], v[184:187]\n"
      "ds_read_b128 a[64:67], v14 offset:22528\n"
      "v_mfma_f32_16x16x32_bf16 v[188:191], v[36:39], a[72:75], v[188:191]\n"
      "v_mfma_f32_16x16x32_bf16 v[184:187], v[40:43], a[76:79], v[184:187]\n"
      "ds_read_b128 a[68:71], v14 offset:23552\n"
      "v_mfma_f32_16x16x32_bf16 v[188:191], v[44:47], a[76:79], v[188:191]\n"
      "s_waitcnt lgkmcnt(4)\n"
      "v_mfma_f32_16x16x32_bf16 v[192:195], a[112:115], a[80:83], 0\n"
      "ds_read_b128 a[72:75], v14 offset:24576\n"
      "v_mfma_f32_16x16x32_bf16 v[196:199], a[116:119], a[80:83], 0\n"
      "v_mfma_f32_16x16x32_bf16 v[192:195], a[120:123], a[84:87], v[192:195]\n"
      "ds_read_b128 a[76:79], v14 offset:25600\n"
      "v_mfma_f32_16x16x32_bf16 v[196:199], a[124:127], a[84:87], v[196:199]\n"
      "s_add_u32 s80, s58, 16384\n"
      "s_add_u32 m0, s71, 147456\n"
      "v_mfma_f32_16x16x32_bf16 v[192:195], a[128:131], a[88:91], v[192:195]\n"
      "ds_read_b128 a[80:83], v14 offset:26624\n"
      "v_mfma_f32_16x16x32_bf16 v[196:199], a[132:135], a[88:91], v[196:199]\n"
      "buffer_load_dwordx4 v170, s[40:43], s80 offen offset:0 sc1 lds\n"
      "v_mfma_f32_16x16x32_bf16 v[192:195], a[136:139], a[92:95], v[192:195]\n"
      "ds_read_b128 a[84:87], v14 offset:27648\n"
      "v_mfma_f32_16x16x32_bf16 v[196:199], a[140:143], a[92:95], v[196:199]\n"
      "s_waitcnt lgkmcnt(4)\n"
      "v_mfma_f32_16x16x32_bf16 v[192:195], a[144:147], a[96:99], v[192:195]\n"
      "ds_read_b128 a[88:91], v14 offset:28672\n"
      "v_mfma_f32_16x16x32_bf16 v[196:199], a[148:151], a[96:99], v[196:199]\n"
      "buffer_load_dwordx4 v170, s[40:43], s80 offen offset:1024 sc1 lds\n"
      "v_mfma_f32_16x16x32_bf16 v[192:195], a[152:155], a[100:103], v[192:195]\n"
      "ds_read_b128 a[92:95], v14 offset:29696\n"
      "v_mfma_f32_16x16x32_bf16 v[196:199], a[156:159], a[100:103], v[196:199]\n"
      "v_mfma_f32_16x16x32_bf16 v[192:195], a[160:163], a[64:67], v[192:195]\n"
      "ds_read_b128 a[96:99], v14 offset:30720\n"
      "v_mfma_f32_16x16x32_bf16 v[196:199], a[164:167], a[64:67], v[196:199]\n"
      "buffer_load_dwordx4 v170, s[40:43], s80 offen offset:2048 sc1 lds\n"
      "v_mfma_f32_16x16x32_bf16 v[192:195], a[168:171], a[68:71], v[192:195]\n"
      "ds_read_b128 a[100:103], v14 offset:31744\n"
      "v_mfma_f32_16x16x32_bf16 v[196:199], a[172:175], a[68:71], v[196:199]\n"
      "s_waitcnt lgkmcnt(4)\n"
      "v_mfma_f32_16x16x32_bf16 v[192:195], a[176:179], a[72:75], v[192:195]\n"
      "s_waitcnt vmcnt(7)\n"
      "s_barrier\n"
      "ds_read_b128 a[64:67], v14 offset:32768\n"
      "v_mfma_f32_16x16x32_bf16 v[196:199], a[180:183], a[72:75], v[196:199]\n"
      "buffer_load_dwordx4 v170, s[40:43], s80 offen offset:3072 sc1 lds\n"
      "v_mfma_f32_16x16x32_bf16 v[192:195], a[184:187], a[76:79], v[192:195]\n"
      "ds_read_b128 a[68:71], v14 offset:33792\n"
      "v_mfma_f32_16x16x32_bf16 v[196:199], a[188:191], a[76:79], v[196:199]\n"
      "v_mfma_f32_16x16x32_bf16 v[192:195], a[192:195], a[80:83], v[192:195]\n"
      "ds_read_b128 a[72:75], v14 offset:34816\n"
      "v_mfma_f32_16x16x32_bf16 v[196:199], a[196:199], a[80:83], v[196:199]\n"
      "v_mfma_f32_16x16x32_bf16 v[192:195], a[200:203], a[84:87], v[192:195]\n"
      "ds_read_b128 a[76:79], v14 offset:35840\n"
      "v_mfma_f32_16x16x32_bf16 v[196:199], a[204:207], a[84:87], v[196:199]\n"
      "s_waitcnt lgkmcnt(4)\n"
      "v_mfma_f32_16x16x32_bf16 v[192:195], a[208:211], a[88:91], v[192:195]\n"
      "ds_read_b128 a[80:83], v14 offset:36864\n"
      "v_mfma_f32_16x16x32_bf16 v[196:199], a[212:215], a[88:91], v[196:199]\n"
      "v_mfma_f32_16x16x32_bf16 v[192:195], a[216:219], a[92:95], v[192:195]\n"
      "ds_read_b128 a[84:87], v14 offset:37888\n"
      "v_mfma_f32_16x16x32_bf16 v[196:199], a[220:223], a[92:95], v[196:199]\n"
      "v_mfma_f32_16x16x32_bf16 v[192:195], a[224:227], a[96:99], v[192:195]\n"
      "ds_read_b128 a[88:91], v14 offset:38912\n"
      "v_mfma_f32_16x16x32_bf16 v[196:199], a[228:231], a[96:99], v[196:199]\n"
      "v_mfma_f32_16x16x32_bf16 v[192:195], a[232:235], a[100:103], v[192:195]\n"
      "ds_read_b128 a[92:95], v14 offset:39936\n"
      "v_mfma_f32_16x16x32_bf16 v[196:199], a[236:239], a[100:103], v[196:199]\n"
      "s_waitcnt lgkmcnt(4)\n"
      "v_mfma_f32_16x16x32_bf16 v[192:195], a[240:243], a[64:67], v[192:195]\n"
      "ds_read_b128 a[96:99], v14 offset:40960\n"
      "v_mfma_f32_16x16x32_bf16 v[196:199], a[244:247], a[64:67], v[196:199]\n"
      "v_mfma_f32_16x16x32_bf16 v[192:195], a[248:251], a[68:71], v[192:195]\n"
      "ds_read_b128 a[100:103], v14 offset:41984\n"
      "v_mfma_f32_16x16x32_bf16 v[196:199], a[252:255], a[68:71], v[196:199]\n"
      "v_perm_b32 v128, v176, v182, s89\n"
      "v_perm_b32 v129, v177, v182, s90\n"
      "v_perm_b32 v130, v178, v182, s91\n"
      "buffer_store_dwordx4 v[128:131], v170, s[40:43], s94 offen offset:0 sc1\n"
      "v_mfma_f32_16x16x32_bf16 v[192:195], a[0:3], a[72:75], v[192:195]\n"
      "ds_read_b128 a[64:67], v14 offset:43008\n"
      "v_mfma_f32_16x16x32_bf16 v[196:199], a[4:7], a[72:75], v[196:199]\n"
      "v_mfma_f32_16x16x32_bf16 v[192:195], a[8:11], a[76:79], v[192:195]\n"
      "ds_read_b128 a[68:71], v14 offset:44032\n"
      "v_mfma_f32_16x16x32_bf16 v[196:199], a[12:15], a[76:79], v[196:199]\n"
      "s_waitcnt lgkmcnt(4)\n"
      "v_mfma_f32_16x16x32_bf16 v[192:195], a[16:19], a[80:83], v[192:195]\n"
      "ds_read_b128 a[72:75], v14 offset:45056\n"
      "v_mfma_f32_16x16x32_bf16 v[196:199], a[20:23], a[80:83], v[196:199]\n"
      "v_perm_b32 v132, v179, v183, s89\n"
      "v_perm_b32 v133, v180, v183, s90\n"
      "v_perm_b32 v134, v181, v183, s91\n"
      "buffer_store_dwordx4 v[132:135], v170, s[40:43], s94 offen offset:1024 sc1\n"
      "v_mfma_f32_16x16x32_bf16 v[192:195], a[24:27], a[84:87], v[192:195]\n"
      "ds_read_b128 a[76:79], v14 offset:46080\n"
      "v_mfma_f32_16x16x32_bf16 v[196:199], a[28:31], a[84:87], v[196:199]\n"
      "v_mfma_f32_16x16x32_bf16 v[192:195], v[208:211], a[88:91], v[192:195]\n"
      "ds_read_b128 a[80:83], v14 offset:47104\n"
      "v_mfma_f32_16x16x32_bf16 v[196:199], v[212:215], a[88:91], v[196:199]\n"
      "v_mfma_f32_16x16x32_bf16 v[192:195], v[216:219], a[92:95], v[192:195]\n"
      "ds_read_b128 a[84:87], v14 offset:48128\n"
      "v_mfma_f32_16x16x32_bf16 v[196:199], v[220:223], a[92:95], v[196:199]\n"
      "v_perm_b32 v136, v184, v190, s89\n"
      "v_perm_b32 v137, v185, v190, s90\n"
      "v_perm_b32 v138, v186, v190, s91\n"
      "buffer_store_dwordx4 v[136:139], v170, s[40:43], s95 offen offset:0 sc1\n"
      "s_waitcnt lgkmcnt(4)\n"
      "v_mfma_f32_16x16x32_bf16 v[192:195], v[224:227], a[96:99], v[192:195]\n"
      "s_waitcnt vmcnt(7)\n"
      "s_barrier\n"
      "ds_read_b128 a[88:91], v9 offset:0\n"
      "v_mfma_f32_16x16x32_bf16 v[196:199], v[228:231], a[96:99], v[196:199]\n"
      "v_mfma_f32_16x16x32_bf16 v[192:195], v[232:235], a[100:103], v[192:195]\n"
      "ds_read_b128 a[92:95], v9 offset:1024\n"
      "v_mfma_f32_16x16x32_bf16 v[196:199], v[236:239], a[100:103], v[196:199]\n"
      "v_mfma_f32_16x16x32_bf16 v[192:195], v[240:243], a[64:67], v[192:195]\n"
      "ds_read_b128 a[96:99], v9 offset:2048\n"
      "v_mfma_f32_16x16x32_bf16 v[196:199], v[244:247], a[64:67], v[196:199]\n"
      "v_perm_b32 v140, v187, v191, s89\n"
      "v_perm_b32 v141, v188, v191, s90\n"
      "v_perm_b32 v142, v189, v191, s91\n"
      "buffer_store_dwordx4 v[140:143], v170, s[40:43], s95 offen offset:1024 sc1\n"
      "v_mfma_f32_16x16x32_bf16 v[192:195], v[248:251], a[68:71], v[192:195]\n"
      "ds_read_b128 a[100:103], v9 offset:3072\n"
      "v_mfma_f32_16x16x32_bf16 v[196:199], v[252:255], a[68:71], v[196:199]\n"
      "s_waitcnt lgkmcnt(4)\n"
      "v_mfma_f32_16x16x32_bf16 v[192:195], v[16:19], a[72:75], v[192:195]\n"
      "ds_read_b128 a[64:67], v9 offset:4096\n"
      "v_mfma_f32_16x16x32_bf16 v[196:199], v[20:23], a[72:75], v[196:199]\n"
      "v_mfma_f32_16x16x32_bf16 v[192:195], v[24:27], a[76:79], v[192:195]\n"
      "ds_read_b128 a[68:71], v9 offset:5120\n"
      "v_mfma_f32_16x16x32_bf16 v[196:199], v[28:31], a[76:79], v[196:199]\n"
      "v_mfma_f32_16x16x32_bf16 v[192:195], v[32:35], a[80:83], v[192:195]\n"
      "ds_read_b128 a[72:75], v9 offset:6144\n"
      "v_mfma_f32_16x16x32_bf16 v[196:199], v[36:39], a[80:83], v[196:199]\n"
      "v_mfma_f32_16x16x32_bf16 v[192:195], v[40:43], a[84:87], v[192:195]\n"
      "ds_read_b128 a[76:79], v9 offset:7168\n"
      "v_mfma_f32_16x16x32_bf16 v[196:199], v[44:47], a[84:87], v[196:199]\n"
      "s_waitcnt lgkmcnt(4)\n"
      "v_mfma_f32_16x16x32_bf16 v[200:203], a[112:115], a[88:91], 0\n"
      "ds_read_b128 a[80:83], v9 offset:8192\n"
      "v_mfma_f32_16x16x32_bf16 v[204:207], a[116:119], a[88:91], 0\n"
      "v_mfma_f32_16x16x32_bf16 v[200:203], a[120:123], a[92:95], v[200:203]\n"
      "ds_read_b128 a[84:87], v9 offset:9216\n"
      "v_mfma_f32_16x16x32_bf16 v[204:207], a[124:127], a[92:95], v[204:207]\n"
      "s_nop 0\n"
      "v_perm_b32 v144, v192, v198, s89\n"
      "v_perm_b32 v145, v193, v198, s90\n"
      "v_perm_b32 v146, v194, v198, s91\n"
      "buffer_store_dwordx4 v[144:147], v170, s[40:43], s59 offen offset:0 sc1\n"
      "v_mfma_f32_16x16x32_bf16 v[200:203], a[128:131], a[96:99], v[200:203]\n"
      "ds_read_b128 a[88:91], v9 offset:10240\n"
      "v_mfma_f32_16x16x32_bf16 v[204:207], a[132:135], a[96:99], v[204:207]\n"
      "v_mfma_f32_16x16x32_bf16 v[200:203], a[136:139], a[100:103], v[200:203]\n"
      "ds_read_b128 a[92:95], v9 offset:11264\n"
      "v_mfma_f32_16x16x32_bf16 v[204:207], a[140:143], a[100:103], v[204:207]\n"
      "v_perm_b32 v148, v195, v199, s89\n"
      "v_perm_b32 v149, v196, v199, s90\n"
      "v_perm_b32 v150, v197, v199, s91\n"
      "buffer_store_dwordx4 v[148:151], v170, s[40:43], s59 offen offset:1024 sc1\n"
      "s_waitcnt lgkmcnt(4)\n"
      "v_mfma_f32_16x16x32_bf16 v[200:203], a[144:147], a[64:67], v[200:203]\n"
      "ds_read_b128 a[96:99], v9 offset:12288\n"
      "v_mfma_f32_16x16x32_bf16 v[204:207], a[148:151], a[64:67], v[204:207]\n"
      "v_mfma_f32_16x16x32_bf16 v[200:203], a[152:155], a[68:71], v[200:203]\n"
      "ds_read_b128 a[100:103], v9 offset:13312\n"
      "v_mfma_f32_16x16x32_bf16 v[204:207], a[156:159], a[68:71], v[204:207]\n"
      "v_mfma_f32_16x16x32_bf16 v[200:203], a[160:163], a[72:75], v[200:203]\n"
      "ds_read_b128 a[64:67], v9 offset:14336\n"
      "v_mfma_f32_16x16x32_bf16 v[204:207], a[164:167], a[72:75], v[204:207]\n"
      "v_mfma_f32_16x16x32_bf16 v[200:203], a[168:171], a[76:79], v[200:203]\n"
      "ds_read_b128 a[68:71], v9 offset:15360\n"
      "v_mfma_f32_16x16x32_bf16 v[204:207], a[172:175], a[76:79], v[204:207]\n"
      "s_waitcnt lgkmcnt(4)\n"
      "v_mfma_f32_16x16x32_bf16 v[200:203], a[176:179], a[80:83], v[200:203]\n"
      "s_waitcnt vmcnt(6)\n"
      "s_barrier\n"
      "ds_read_b128 a[72:75], v9 offset:16384\n"
      "v_mfma_f32_16x16x32_bf16 v[204:207], a[180:183], a[80:83], v[204:207]\n"
      "v_mfma_f32_16x16x32_bf16 v[200:203], a[184:187], a[84:87], v[200:203]\n"
      "ds_read_b128 a[76:79], v9 offset:17408\n"
      "v_mfma_f32_16x16x32_bf16 v[204:207], a[188:191], a[84:87], v[204:207]\n"
      "v_mfma_f32_16x16x32_bf16 v[200:203], a[192:195], a[88:91], v[200:203]\n"
      "ds_read_b128 a[80:83], v9 offset:18432\n"
      "v_mfma_f32_16x16x32_bf16 v[204:207], a[196:199], a[88:91], v[204:207]\n"
      "v_mfma_f32_16x16x32_bf16 v[200:203], a[200:203], a[92:95], v[200:203]\n"
      "ds_read_b128 a[84:87], v9 offset:19456\n"
      "v_mfma_f32_16x16x32_bf16 v[204:207], a[204:207], a[92:95], v[204:207]\n"
      "s_waitcnt lgkmcnt(4)\n"
      "v_mfma_f32_16x16x32_bf16 v[200:203], a[208:211], a[96:99], v[200:203]\n"
      "ds_read_b128 a[88:91], v9 offset:20480\n"
      "v_mfma_f32_16x16x32_bf16 v[204:207], a[212:215], a[96:99], v[204:207]\n"
      "v_mfma_f32_16x16x32_bf16 v[200:203], a[216:219], a[100:103], v[200:203]\n"
      "ds_read_b128 a[92:95], v9 offset:21504\n"
      "v_mfma_f32_16x16x32_bf16 v[204:207], a[220:223], a[100:103], v[204:207]\n"
      "v_mfma_f32_16x16x32_bf16 v[200:203], a[224:227], a[64:67], v[200:203]\n"
      "ds_read_b128 a[96:99], v9 offset:22528\n"
      "v_mfma_f32_16x16x32_bf16 v[204:207], a[228:231], a[64:67], v[204:207]\n"
      "v_mfma_f32_16x16x32_bf16 v[200:203], a[232:235], a[68:71], v[200:203]\n"
      "ds_read_b128 a[100:103], v9 offset:23552\n"
      "v_mfma_f32_16x16x32_bf16 v[204:207], a[236:239], a[68:71], v[204:207]\n"
      "s_waitcnt lgkmcnt(4)\n"
      "v_mfma_f32_16x16x32_bf16 v[200:203], a[240:243], a[72:75], v[200:203]\n"
      "ds_read_b128 a[64:67], v9 offset:24576\n"
      "v_mfma_f32_16x16x32_bf16 v[204:207], a[244:247], a[72:75], v[204:207]\n"
      "v_mfma_f32_16x16x32_bf16 v[200:203], a[248:251], a[76:79], v[200:203]\n"
      "ds_read_b128 a[68:71], v9 offset:25600\n"
      "v_mfma_f32_16x16x32_bf16 v[204:207], a[252:255], a[76:79], v[204:207]\n"
      "v_mfma_f32_16x16x32_bf16 v[200:203], a[0:3], a[80:83], v[200:203]\n"
      "ds_read_b128 a[72:75], v9 offset:26624\n"
      "v_mfma_f32_16x16x32_bf16 v[204:207], a[4:7], a[80:83], v[204:207]\n"
      "v_mfma_f32_16x16x32_bf16 v[200:203], a[8:11], a[84:87], v[200:203]\n"
      "ds_read_b128 a[76:79], v9 offset:27648\n"
      "v_mfma_f32_16x16x32_bf16 v[204:207], a[12:15], a[84:87], v[204:207]\n"
      "s_waitcnt lgkmcnt(4)\n"
      "v_mfma_f32_16x16x32_bf16 v[200:203], a[16:19], a[88:91], v[200:203]\n"
      "ds_read_b128 a[80:83], v9 offset:28672\n"
      "v_mfma_f32_16x16x32_bf16 v[204:207], a[20:23], a[88:91], v[204:207]\n"
      "v_mfma_f32_16x16x32_bf16 v[200:203], a[24:27], a[92:95], v[200:203]\n"
      "ds_read_b128 a[84:87], v9 offset:29696\n"
      "v_mfma_f32_16x16x32_bf16 v[204:207], a[28:31], a[92:95], v[204:207]\n"
      "v_mfma_f32_16x16x32_bf16 v[200:203], v[208:211], a[96:99], v[200:203]\n"
      "ds_read_b128 a[88:91], v9 offset:30720\n"
      "v_mfma_f32_16x16x32_bf16 v[204:207], v[212:215], a[96:99], v[204:207]\n"
      "v_mfma_f32_16x16x32_bf16 v[200:203], v[216:219], a[100:103], v[200:203]\n"
      "ds_read_b128 a[92:95], v9 offset:31744\n"
      "v_mfma_f32_16x16x32_bf16 v[204:207], v[220:223], a[100:103], v[204:207]\n"
      "s_waitcnt lgkmcnt(4)\n"
      "v_mfma_f32_16x16x32_bf16 v[200:203], v[224:227], a[64:67], v[200:203]\n"
      "v_mfma_f32_16x16x32_bf16 v[204:207], v[228:231], a[64:67], v[204:207]\n"
      "v_mfma_f32_16x16x32_bf16 v[200:203], v[232:235], a[68:71], v[200:203]\n"
      "v_mfma_f32_16x16x32_bf16 v[204:207], v[236:239], a[68:71], v[204:207]\n"
      "v_mfma_f32_16x16x32_bf16 v[200:203], v[240:243], a[72:75], v[200:203]\n"
      "v_mfma_f32_16x16x32_bf16 v[204:207], v[244:247], a[72:75], v[204:207]\n"
      "v_mfma_f32_16x16x32_bf16 v[200:203], v[248:251], a[76:79], v[200:203]\n"
      "v_mfma_f32_16x16x32_bf16 v[204:207], v[252:255], a[76:79], v[204:207]\n"
      "s_waitcnt lgkmcnt(0)\n"
      "v_mfma_f32_16x16x32_bf16 v[200:203], v[16:19], a[80:83], v[200:203]\n"
      "v_mfma_f32_16x16x32_bf16 v[204:207], v[20:23], a[80:83], v[204:207]\n"
      "v_mfma_f32_16x16x32_bf16 v[200:203], v[24:27], a[84:87], v[200:203]\n"
      "v_mfma_f32_16x16x32_bf16 v[204:207], v[28:31], a[84:87], v[204:207]\n"
      "v_mfma_f32_16x16x32_bf16 v[200:203], v[32:35], a[88:91], v[200:203]\n"
      "v_mfma_f32_16x16x32_bf16 v[204:207], v[36:39], a[88:91], v[204:207]\n"
      "v_mfma_f32_16x16x32_bf16 v[200:203], v[40:43], a[92:95], v[200:203]\n"
      "v_mfma_f32_16x16x32_bf16 v[204:207], v[44:47], a[92:95], v[204:207]\n"
      "buffer_load_dwordx4 v[80:83], v170, s[40:43], s75 offen offset:0 sc1\n"
      "buffer_load_dwordx4 v[84:87], v170, s[40:43], s75 offen offset:1024 sc1\n"
      "buffer_load_dwordx4 v[72:75], v170, s[40:43], s74 offen offset:0 sc1\n"
      "buffer_load_dwordx4 v[76:79], v170, s[40:43], s74 offen offset:1024 sc1\n"
      "buffer_load_dwordx4 v[64:67], v170, s[40:43], s73 offen offset:0 sc1\n"
      "buffer_load_dwordx4 v[68:71], v170, s[40:43], s73 offen offset:1024 sc1\n"
      "s_mov_b32 s87, 65536\n"
      ".Lgq_p_spin:\n"
      "s_waitcnt vmcnt(0)\n"
      "v_min3_u32 v2, v67, v71, v75\n"
      "v_max3_u32 v3, v67, v71, v75\n"
      "v_min3_u32 v2, v2, v79, v83\n"
      "v_max3_u32 v3, v3, v79, v83\n"
      "v_min_u32 v2, v2, v87\n"
      "v_max_u32 v3, v3, v87\n"
      "s_mov_b32 s54, s48\n"
      "s_mov_b32 s55, s48\n"
      "v_cmp_eq_u64 vcc, s[54:55], v[2:3]\n"
      "s_nop 1\n"
      "s_cmp_eq_u64 vcc, exec\n"
      "s_cbranch_scc1 .Lgq_p_ok\n"
      "buffer_load_dwordx4 v[80:83], v170, s[40:43], s75 offen offset:0 sc1\n"
      "buffer_load_dwordx4 v[84:87], v170, s[40:43], s75 offen offset:1024 sc1\n"
      "buffer_load_dwordx4 v[72:75], v170, s[40:43], s74 offen offset:0 sc1\n"
      "buffer_load_dwordx4 v[76:79], v170, s[40:43], s74 offen offset:1024 sc1\n"
      "buffer_load_dwordx4 v[64:67], v170, s[40:43], s73 offen offset:0 sc1\n"
      "buffer_load_dwordx4 v[68:71], v170, s[40:43], s73 offen offset:1024 sc1\n"
      "s_sub_u32 s87, s87, 1\n"
      "s_cmp_lg_u32 s87, 0\n"
      "s_cbranch_scc1 .Lgq_p_spin\n"
      "s_waitcnt vmcnt(0)\n"
      ".Lgq_p_ok:\n"
      "v_perm_b32 v67, v65, v64, s92\n"
      "v_lshl_or_b32 v67, v66, 24, v67\n"
      "v_add_f32 v200, v200, v64\n"
      "v_add_f32 v201, v201, v65\n"
      "v_add_f32 v202, v202, v66\n"
      "v_add_f32 v206, v206, v67\n"
      "v_perm_b32 v71, v69, v68, s92\n"
      "v_lshl_or_b32 v71, v70, 24, v71\n"
      "v_add_f32 v203, v203, v68\n"
      "v_add_f32 v204, v204, v69\n"
      "v_add_f32 v205, v205, v70\n"
      "v_add_f32 v207, v207, v71\n"
      "v_perm_b32 v75, v73, v72, s92\n"
      "v_lshl_or_b32 v75, v74, 24, v75\n"
      "v_add_f32 v200, v200, v72\n"
      "v_add_f32 v201, v201, v73\n"
      "v_add_f32 v202, v202, v74\n"
      "v_add_f32 v206, v206, v75\n"
      "v_perm_b32 v79, v77, v76, s92\n"
      "v_lshl_or_b32 v79, v78, 24, v79\n"
      "v_add_f32 v203, v203, v76\n"
      "v_add_f32 v204, v204, v77\n"
      "v_add_f32 v205, v205, v78\n"
      "v_add_f32 v207, v207, v79\n"
      "v_perm_b32 v83, v81, v80, s92\n"
      "v_lshl_or_b32 v83, v82, 24, v83\n"
      "v_add_f32 v200, v200, v80\n"
      "v_add_f32 v201, v201, v81\n"
      "v_add_f32 v202, v202, v82\n"
      "v_add_f32 v206, v206, v83\n"
      "v_perm_b32 v87, v85, v84, s92\n"
      "v_lshl_or_b32 v87, v86, 24, v87\n"
      "v_add_f32 v203, v203, v84\n"
      "v_add_f32 v204, v204, v85\n"
      "v_add_f32 v205, v205, v86\n"
      "v_add_f32 v207, v207, v87\n"
      "v_cvt_pk_bf16_f32 v200, v200, v201\n"
      "v_cvt_pk_bf16_f32 v201, v202, v203\n"
      "v_cvt_pk_bf16_f32 v202, v204, v205\n"
      "v_cvt_pk_bf16_f32 v203, v206, v207\n"
      "buffer_store_dwordx4 v[200:203], v15, s[44:47], s65 offen\n"
      "s_branch .Lgq_end\n"
      ".Lgq_qload:\n"
      "s_add_u32 s80, s91, 0\n"
      "v_mfma_f32_32x32x16_bf16 v[16:31], a[52:55], a[64:67], v[16:31]\n"
      "v_mfma_f32_32x32x16_bf16 v[32:47], a[56:59], a[68:71], v[32:47]\n"
      "v_mfma_f32_32x32x16_bf16 v[16:31], a[32:35], a[72:75], v[16:31]\n"
      "v_mfma_f32_32x32x16_bf16 v[32:47], a[36:39], a[76:79], v[32:47]\n"
      "buffer_load_dwordx4 v[176:179], v170, s[40:43], s80 offen offset:0 sc1\n"
      "v_mfma_f32_32x32x16_bf16 v[16:31], a[40:43], a[64:67], v[16:31]\n"
      "v_mfma_f32_32x32x16_bf16 v[32:47], a[44:47], a[68:71], v[32:47]\n"
      "v_mfma_f32_32x32x16_bf16 v[16:31], a[48:51], a[72:75], v[16:31]\n"
      "v_mfma_f32_32x32x16_bf16 v[32:47], a[52:55], a[76:79], v[32:47]\n"
      "buffer_load_dwordx4 v[180:183], v170, s[40:43], s80 offen offset:1024 sc1\n"
      "v_mfma_f32_32x32x16_bf16 v[16:31], a[56:59], a[64:67], v[16:31]\n"
      "v_mfma_f32_32x32x16_bf16 v[32:47], a[32:35], a[68:71], v[32:47]\n"
      "v_mfma_f32_32x32x16_bf16 v[16:31], a[36:39], a[72:75], v[16:31]\n"
      "v_mfma_f32_32x32x16_bf16 v[32:47], a[40:43], a[76:79], v[32:47]\n"
      "buffer_load_dwordx4 v[48:51], v170, s[40:43], s80 offen offset:2048 sc1\n"
      "s_add_u32 s80, s91, 4096\n"
      "v_mfma_f32_32x32x16_bf16 v[16:31], a[44:47], a[64:67], v[16:31]\n"
      "v_mfma_f32_32x32x16_bf16 v[32:47], a[48:51], a[68:71], v[32:47]\n"
      "v_mfma_f32_32x32x16_bf16 v[16:31], a[52:55], a[72:75], v[16:31]\n"
      "v_mfma_f32_32x32x16_bf16 v[32:47], a[56:59], a[76:79], v[32:47]\n"
      "buffer_load_dwordx4 v[184:187], v170, s[40:43], s80 offen offset:0 sc1\n"
      "v_mfma_f32_32x32x16_bf16 v[16:31], a[32:35], a[64:67], v[16:31]\n"
      "v_mfma_f32_32x32x16_bf16 v[32:47], a[36:39], a[68:71], v[32:47]\n"
      "v_mfma_f32_32x32x16_bf16 v[16:31], a[40:43], a[72:75], v[16:31]\n"
      "v_mfma_f32_32x32x16_bf16 v[32:47], a[44:47], a[76:79], v[32:47]\n"
      "buffer_load_dwordx4 v[188:191], v170, s[40:43], s80 offen offset:1024 sc1\n"
      "v_mfma_f32_32x32x16_bf16 v[16:31], a[48:51], a[64:67], v[16:31]\n"
      "v_mfma_f32_32x32x16_bf16 v[32:47], a[52:55], a[68:71], v[32:47]\n"
      "v_mfma_f32_32x32x16_bf16 v[16:31], a[56:59], a[72:75], v[16:31]\n"
      "v_mfma_f32_32x32x16_bf16 v[32:47], a[32:35], a[76:79], v[32:47]\n"
      "buffer_load_dwordx4 v[52:55], v170, s[40:43], s80 offen offset:2048 sc1\n"
      "s_add_u32 s80, s91, 8192\n"
      "v_mfma_f32_32x32x16_bf16 v[16:31], a[36:39], a[64:67], v[16:31]\n"
      "v_mfma_f32_32x32x16_bf16 v[32:47], a[40:43], a[68:71], v[32:47]\n"
      "v_mfma_f32_32x32x16_bf16 v[16:31], a[44:47], a[72:75], v[16:31]\n"
      "v_mfma_f32_32x32x16_bf16 v[32:47], a[48:51], a[76:79], v[32:47]\n"
      "buffer_load_dwordx4 v[192:195], v170, s[40:43], s80 offen offset:0 sc1\n"
      "v_mfma_f32_32x32x16_bf16 v[16:31], a[52:55], a[64:67], v[16:31]\n"
      "v_mfma_f32_32x32x16_bf16 v[32:47], a[56:59], a[68:71], v[32:47]\n"
      "v_mfma_f32_32x32x16_bf16 v[16:31], a[32:35], a[72:75], v[16:31]\n"
      "v_mfma_f32_32x32x16_bf16 v[32:47], a[36:39], a[76:79], v[32:47]\n"
      "buffer_load_dwordx4 v[196:199], v170, s[40:43], s80 offen offset:1024 sc1\n"
      "v_mfma_f32_32x32x16_bf16 v[16:31], a[40:43], a[64:67], v[16:31]\n"
      "v_mfma_f32_32x32x16_bf16 v[32:47], a[44:47], a[68:71], v[32:47]\n"
      "v_mfma_f32_32x32x16_bf16 v[16:31], a[48:51], a[72:75], v[16:31]\n"
      "v_mfma_f32_32x32x16_bf16 v[32:47], a[52:55], a[76:79], v[32:47]\n"
      "buffer_load_dwordx4 v[56:59], v170, s[40:43], s80 offen offset:2048 sc1\n"
      "s_add_u32 s80, s91, 12288\n"
      "v_mfma_f32_32x32x16_bf16 v[16:31], a[56:59], a[64:67], v[16:31]\n"
      "v_mfma_f32_32x32x16_bf16 v[32:47], a[32:35], a[68:71], v[32:47]\n"
      "v_mfma_f32_32x32x16_bf16 v[16:31], a[36:39], a[72:75], v[16:31]\n"
      "v_mfma_f32_32x32x16_bf16 v[32:47], a[40:43], a[76:79], v[32:47]\n"
      "buffer_load_dwordx4 v[200:203], v170, s[40:43], s80 offen offset:0 sc1\n"
      "v_mfma_f32_32x32x16_bf16 v[16:31], a[44:47], a[64:67], v[16:31]\n"
      "v_mfma_f32_32x32x16_bf16 v[32:47], a[48:51], a[68:71], v[32:47]\n"
      "v_mfma_f32_32x32x16_bf16 v[16:31], a[52:55], a[72:75], v[16:31]\n"
      "v_mfma_f32_32x32x16_bf16 v[32:47], a[56:59], a[76:79], v[32:47]\n"
      "buffer_load_dwordx4 v[204:207], v170, s[40:43], s80 offen offset:1024 sc1\n"
      "v_mfma_f32_32x32x16_bf16 v[16:31], a[32:35], a[64:67], v[16:31]\n"
      "v_mfma_f32_32x32x16_bf16 v[32:47], a[36:39], a[68:71], v[32:47]\n"
      "v_mfma_f32_32x32x16_bf16 v[16:31], a[40:43], a[72:75], v[16:31]\n"
      "v_mfma_f32_32x32x16_bf16 v[32:47], a[44:47], a[76:79], v[32:47]\n"
      "buffer_load_dwordx4 v[60:63], v170, s[40:43], s80 offen offset:2048 sc1\n"
      "s_add_u32 s50, s50, 4\n"
      "s_addc_u32 s51, s51, 0\n"
      "s_setpc_b64 s[50:51]\n"
      ".Lgq_qchk:\n"
      "v_min3_u32 v2, v179, v183, v51\n"
      "v_max3_u32 v3, v179, v183, v51\n"
      "v_min3_u32 v2, v2, v187, v191\n"
      "v_max3_u32 v3, v3, v187, v191\n"
      "v_min3_u32 v2, v2, v55, v195\n"
      "v_max3_u32 v3, v3, v55, v195\n"
      "v_min3_u32 v2, v2, v199, v59\n"
      "v_max3_u32 v3, v3, v199, v59\n"
      "v_min3_u32 v2, v2, v203, v207\n"
      "v_max3_u32 v3, v3, v203, v207\n"
      "v_min_u32 v2, v2, v63\n"
      "v_max_u32 v3, v3, v63\n"
      "s_mov_b32 s54, s48\n"
      "s_mov_b32 s55, s48\n"
      "v_cmp_eq_u64 vcc, s[54:55], v[2:3]\n"
      "s_nop 1\n"
      "s_cmp_eq_u64 vcc, exec\n"
      "s_cselect_b32 s86, 1, 0\n"
      "s_add_u32 s50, s50, 4\n"
      "s_addc_u32 s51, s51, 0\n"
      "s_setpc_b64 s[50:51]\n"
      ".Lgq_rescale0:\n"
      "s_nop 7\n"
      "s_nop 7\n"
      "v_mov_b32 v6, v173\n"
      "v_mov_b32 v7, v173\n"
      "s_nop 1\n"
      "v_permlane32_swap_b32 v6, v7\n"
      "v_max_f32 v173, v6, v7\n"
      "v_max_f32 v173, 0, v173\n"
      "v_exp_f32 v6, -v173\n"
      "v_sub_f32 v16, v16, v173\n"
      "v_sub_f32 v17, v17, v173\n"
      "v_sub_f32 v18, v18, v173\n"
      "v_sub_f32 v19, v19, v173\n"
      "v_sub_f32 v20, v20, v173\n"
      "v_sub_f32 v21, v21, v173\n"
      "v_sub_f32 v22, v22, v173\n"
      "v_sub_f32 v23, v23, v173\n"
      "v_sub_f32 v24, v24, v173\n"
      "v_sub_f32 v25, v25, v173\n"
      "v_sub_f32 v26, v26, v173\n"
      "v_sub_f32 v27, v27, v173\n"
      "v_sub_f32 v28, v28, v173\n"
      "v_sub_f32 v29, v29, v173\n"
      "v_sub_f32 v30, v30, v173\n"
      "v_sub_f32 v31, v31, v173\n"
      "s_branch .Lgq_rescale\n"
      ".Lgq_rescale1:\n"
      "s_nop 7\n"
      "s_nop 7\n"
      "v_mov_b32 v6, v173\n"
      "v_mov_b32 v7, v173\n"
      "s_nop 1\n"
      "v_permlane32_swap_b32 v6, v7\n"
      "v_max_f32 v173, v6, v7\n"
      "v_max_f32 v173, 0, v173\n"
      "v_exp_f32 v6, -v173\n"
      "v_sub_f32 v32, v32, v173\n"
      "v_sub_f32 v33, v33, v173\n"
      "v_sub_f32 v34, v34, v173\n"
      "v_sub_f32 v35, v35, v173\n"
      "v_sub_f32 v36, v36, v173\n"
      "v_sub_f32 v37, v37, v173\n"
      "v_sub_f32 v38, v38, v173\n"
      "v_sub_f32 v39, v39, v173\n"
      "v_sub_f32 v40, v40, v173\n"
      "v_sub_f32 v41, v41, v173\n"
      "v_sub_f32 v42, v42, v173\n"
      "v_sub_f32 v43, v43, v173\n"
      "v_sub_f32 v44, v44, v173\n"
      "v_sub_f32 v45, v45, v173\n"
      "v_sub_f32 v46, v46, v173\n"
      "v_sub_f32 v47, v47, v173\n"
      ".Lgq_rescale:\n"
      "v_sub_f32 v48, v48, v173\n"
      "v_sub_f32 v49, v49, v173\n"
      "v_sub_f32 v50, v50, v173\n"
      "v_sub_f32 v51, v51, v173\n"
      "v_sub_f32 v52, v52, v173\n"
      "v_sub_f32 v53, v53, v173\n"
      "v_sub_f32 v54, v54, v173\n"
      "v_sub_f32 v55, v55, v173\n"
      "v_sub_f32 v56, v56, v173\n"
      "v_sub_f32 v57, v57, v173\n"
      "v_sub_f32 v58, v58, v173\n"
      "v_sub_f32 v59, v59, v173\n"
      "v_sub_f32 v60, v60, v173\n"
      "v_sub_f32 v61, v61, v173\n"
      "v_sub_f32 v62, v62, v173\n"
      "v_sub_f32 v63, v63, v173\n"
      "v_mul_f32 v64, v6, v64\n"
      "v_mul_f32 v65, v6, v65\n"
      "v_mul_f32 v66, v6, v66\n"
      "v_mul_f32 v67, v6, v67\n"
      "v_mul_f32 v68, v6, v68\n"
      "v_mul_f32 v69, v6, v69\n"
      "v_mul_f32 v70, v6, v70\n"
      "v_mul_f32 v71, v6, v71\n"
      "v_mul_f32 v72, v6, v72\n"
      "v_mul_f32 v73, v6, v73\n"
      "v_mul_f32 v74, v6, v74\n"
      "v_mul_f32 v75, v6, v75\n"
      "v_mul_f32 v76, v6, v76\n"
      "v_mul_f32 v77, v6, v77\n"
      "v_mul_f32 v78, v6, v78\n"
      "v_mul_f32 v79, v6, v79\n"
      "v_mul_f32 v80, v6, v80\n"
      "v_mul_f32 v81, v6, v81\n"
      "v_mul_f32 v82, v6, v82\n"
      "v_mul_f32 v83, v6, v83\n"
      "v_mul_f32 v84, v6, v84\n"
      "v_mul_f32 v85, v6, v85\n"
      "v_mul_f32 v86, v6, v86\n"
      "v_mul_f32 v87, v6, v87\n"
      "v_mul_f32 v88, v6, v88\n"
      "v_mul_f32 v89, v6, v89\n"
      "v_mul_f32 v90, v6, v90\n"
      "v_mul_f32 v91, v6, v91\n"
      "v_mul_f32 v92, v6, v92\n"
      "v_mul_f32 v93, v6, v93\n"
      "v_mul_f32 v94, v6, v94\n"
      "v_mul_f32 v95, v6, v95\n"
      "v_mul_f32 v96, v6, v96\n"
      "v_mul_f32 v97, v6, v97\n"
      "v_mul_f32 v98, v6, v98\n"
      "v_mul_f32 v99, v6, v99\n"
      "v_mul_f32 v100, v6, v100\n"
      "v_mul_f32 v101, v6, v101\n"
      "v_mul_f32 v102, v6, v102\n"
      "v_mul_f32 v103, v6, v103\n"
      "v_mul_f32 v104, v6, v104\n"
      "v_mul_f32 v105, v6, v105\n"
      "v_mul_f32 v106, v6, v106\n"
      "v_mul_f32 v107, v6, v107\n"
      "v_mul_f32 v108, v6, v108\n"
      "v_mul_f32 v109, v6, v109\n"
      "v_mul_f32 v110, v6, v110\n"
      "v_mul_f32 v111, v6, v111\n"
      "v_mul_f32 v112, v6, v112\n"
      "v_mul_f32 v113, v6, v113\n"
      "v_mul_f32 v114, v6, v114\n"
      "v_mul_f32 v115, v6, v115\n"
      "v_mul_f32 v116, v6, v116\n"
      "v_mul_f32 v117, v6, v117\n"
      "v_mul_f32 v118, v6, v118\n"
      "v_mul_f32 v119, v6, v119\n"
      "v_mul_f32 v120, v6, v120\n"
      "v_mul_f32 v121, v6, v121\n"
      "v_mul_f32 v122, v6, v122\n"
      "v_mul_f32 v123, v6, v123\n"
      "v_mul_f32 v124, v6, v124\n"
      "v_mul_f32 v125, v6, v125\n"
      "v_mul_f32 v126, v6, v126\n"
      "v_mul_f32 v127, v6, v127\n"
      "v_mul_f32 v128, v6, v128\n"
      "v_mul_f32 v129, v6, v129\n"
      "s_add_u32 s50, s50, 4\n"
      "s_addc_u32 s51, s51, 0\n"
      "s_setpc_b64 s[50:51]\n"
      ".Lgq_bs0_rare:\n"
      "s_getpc_b64 s[50:51]\n"
      "s_branch .Lgq_rescale1\n"
      "s_branch .Lgq_bs0_ret\n"
      ".Lgq_bs1_rare:\n"
      "s_getpc_b64 s[50:51]\n"
      "s_branch .Lgq_rescale0\n"
      "s_branch .Lgq_bs1_ret\n"
      ".Lgq_bk0_rare:\n"
      "s_getpc_b64 s[50:51]\n"
      "s_branch .Lgq_rescale1\n"
      "s_branch .Lgq_bk0_ret\n"
      ".Lgq_bk1_rare:\n"
      "s_getpc_b64 s[50:51]\n"
      "s_branch .Lgq_rescale0\n"
      "s_branch .Lgq_bk1_ret\n"
      ".Lgq_bp0_rare:\n"
      "s_getpc_b64 s[50:51]\n"
      "s_branch .Lgq_rescale1\n"
      "s_branch .Lgq_bp0_ret\n"
      ".Lgq_bp1_rare:\n"
      "s_getpc_b64 s[50:51]\n"
      "s_branch .Lgq_rescale0\n"
      "s_branch .Lgq_bp1_ret\n"
      ".Lgq_wtab:\n"
      "ds_read_b64_tr_b16 a[128:129], v156 offset:16384\n"
      "ds_read_b64_tr_b16 a[130:131], v157 offset:16384\n"
      "ds_read_b64_tr_b16 a[132:133], v156 offset:16392\n"
      "ds_read_b64_tr_b16 a[134:135], v157 offset:16392\n"
      "ds_read_b64_tr_b16 a[136:137], v156 offset:24576\n"
      "ds_read_b64_tr_b16 a[138:139], v157 offset:24576\n"
      "ds_read_b64_tr_b16 a[140:141], v156 offset:24584\n"
      "ds_read_b64_tr_b16 a[142:143], v157 offset:24584\n"
      "s_setpc_b64 s[50:51]\n"
      "s_nop 0\n"
      "ds_read_b64_tr_b16 a[144:145], v156 offset:16384\n"
      "ds_read_b64_tr_b16 a[146:147], v157 offset:16384\n"
      "ds_read_b64_tr_b16 a[148:149], v156 offset:16392\n"
      "ds_read_b64_tr_b16 a[150:151], v157 offset:16392\n"
      "ds_read_b64_tr_b16 a[152:153], v156 offset:24576\n"
      "ds_read_b64_tr_b16 a[154:155], v157 offset:24576\n"
      "ds_read_b64_tr_b16 a[156:157], v156 offset:24584\n"
      "ds_read_b64_tr_b16 a[158:159], v157 offset:24584\n"
      "s_setpc_b64 s[50:51]\n"
      "s_nop 0\n"
      "ds_read_b64_tr_b16 a[160:161], v156 offset:16384\n"
      "ds_read_b64_tr_b16 a[162:163], v157 offset:16384\n"
      "ds_read_b64_tr_b16 a[164:165], v156 offset:16392\n"
      "ds_read_b64_tr_b16 a[166:167], v157 offset:16392\n"
      "ds_read_b64_tr_b16 a[168:169], v156 offset:24576\n"
      "ds_read_b64_tr_b16 a[170:171], v157 offset:24576\n"
      "ds_read_b64_tr_b16 a[172:173], v156 offset:24584\n"
      "ds_read_b64_tr_b16 a[174:175], v157 offset:24584\n"
      "s_setpc_b64 s[50:51]\n"
      "s_nop 0\n"
      "ds_read_b64_tr_b16 a[176:177], v156 offset:16384\n"
      "ds_read_b64_tr_b16 a[178:179], v157 offset:16384\n"
      "ds_read_b64_tr_b16 a[180:181], v156 offset:16392\n"
      "ds_read_b64_tr_b16 a[182:183], v157 offset:16392\n"
      "ds_read_b64_tr_b16 a[184:185], v156 offset:24576\n"
      "ds_read_b64_tr_b16 a[186:187], v157 offset:24576\n"
      "ds_read_b64_tr_b16 a[188:189], v156 offset:24584\n"
      "ds_read_b64_tr_b16 a[190:191], v157 offset:24584\n"
      "s_setpc_b64 s[50:51]\n"
      "s_nop 0\n"
      "ds_read_b64_tr_b16 a[192:193], v156 offset:16384\n"
      "ds_read_b64_tr_b16 a[194:195], v157 offset:16384\n"
      "ds_read_b64_tr_b16 a[196:197], v156 offset:16392\n"
      "ds_read_b64_tr_b16 a[198:199], v157 offset:16392\n"
      "ds_read_b64_tr_b16 a[200:201], v156 offset:24576\n"
      "ds_read_b64_tr_b16 a[202:203], v157 offset:24576\n"
      "ds_read_b64_tr_b16 a[204:205], v156 offset:24584\n"
      "ds_read_b64_tr_b16 a[206:207], v157 offset:24584\n"
      "s_setpc_b64 s[50:51]\n"
      "s_nop 0\n"
      "ds_read_b64_tr_b16 a[208:209], v156 offset:16384\n"
      "ds_read_b64_tr_b16 a[210:211], v157 offset:16384\n"
      "ds_read_b64_tr_b16 a[212:213], v156 offset:16392\n"
      "ds_read_b64_tr_b16 a[214:215], v157 offset:16392\n"
      "ds_read_b64_tr_b16 a[216:217], v156 offset:24576\n"
      "ds_read_b64_tr_b16 a[218:219], v157 offset:24576\n"
      "ds_read_b64_tr_b16 a[220:221], v156 offset:24584\n"
      "ds_read_b64_tr_b16 a[222:223], v157 offset:24584\n"
      "s_setpc_b64 s[50:51]\n"
      "s_nop 0\n"
      "ds_read_b64_tr_b16 a[224:225], v156 offset:16384\n"
      "ds_read_b64_tr_b16 a[226:227], v157 offset:16384\n"
      "ds_read_b64_tr_b16 a[228:229], v156 offset:16392\n"
      "ds_read_b64_tr_b16 a[230:231], v157 offset:16392\n"
      "ds_read_b64_tr_b16 a[232:233], v156 offset:24576\n"
      "ds_read_b64_tr_b16 a[234:235], v157 offset:24576\n"
      "ds_read_b64_tr_b16 a[236:237], v156 offset:24584\n"
      "ds_read_b64_tr_b16 a[238:239], v157 offset:24584\n"
      "s_setpc_b64 s[50:51]\n"
      "s_nop 0\n"
      "ds_read_b64_tr_b16 a[240:241], v156 offset:16384\n"
      "ds_read_b64_tr_b16 a[242:243], v157 offset:16384\n"
      "ds_read_b64_tr_b16 a[244:245], v156 offset:16392\n"
      "ds_read_b64_tr_b16 a[246:247], v157 offset:16392\n"
      "ds_read_b64_tr_b16 a[248:249], v156 offset:24576\n"
      "ds_read_b64_tr_b16 a[250:251], v157 offset:24576\n"
      "ds_read_b64_tr_b16 a[252:253], v156 offset:24584\n"
      "ds_read_b64_tr_b16 a[254:255], v157 offset:24584\n"
      "s_setpc_b64 s[50:51]\n"
      "s_nop 0\n"
      "ds_read_b64_tr_b16 a[0:1], v156 offset:16384\n"
      "ds_read_b64_tr_b16 a[2:3], v157 offset:16384\n"
      "ds_read_b64_tr_b16 a[4:5], v156 offset:16392\n"
      "ds_read_b64_tr_b16 a[6:7], v157 offset:16392\n"
      "ds_read_b64_tr_b16 a[8:9], v156 offset:24576\n"
      "ds_read_b64_tr_b16 a[10:11], v157 offset:24576\n"
      "ds_read_b64_tr_b16 a[12:13], v156 offset:24584\n"
      "ds_read_b64_tr_b16 a[14:15], v157 offset:24584\n"
      "s_setpc_b64 s[50:51]\n"
      "s_nop 0\n"
      "ds_read_b64_tr_b16 a[16:17], v156 offset:16384\n"
      "ds_read_b64_tr_b16 a[18:19], v157 offset:16384\n"
      "ds_read_b64_tr_b16 a[20:21], v156 offset:16392\n"
      "ds_read_b64_tr_b16 a[22:23], v157 offset:16392\n"
      "ds_read_b64_tr_b16 a[24:25], v156 offset:24576\n"
      "ds_read_b64_tr_b16 a[26:27], v157 offset:24576\n"
      "ds_read_b64_tr_b16 a[28:29], v156 offset:24584\n"
      "ds_read_b64_tr_b16 a[30:31], v157 offset:24584\n"
      "s_setpc_b64 s[50:51]\n"
      "s_nop 0\n"
      "ds_read_b64_tr_b16 v[208:209], v156 offset:16384\n"
      "ds_read_b64_tr_b16 v[210:211], v157 offset:16384\n"
      "ds_read_b64_tr_b16 v[212:213], v156 offset:16392\n"
      "ds_read_b64_tr_b16 v[214:215], v157 offset:16392\n"
      "ds_read_b64_tr_b16 v[216:217], v156 offset:24576\n"
      "ds_read_b64_tr_b16 v[218:219], v157 offset:24576\n"
      "ds_read_b64_tr_b16 v[220:221], v156 offset:24584\n"
      "ds_read_b64_tr_b16 v[222:223], v157 offset:24584\n"
      "s_setpc_b64 s[50:51]\n"
      "s_nop 0\n"
      "ds_read_b64_tr_b16 v[224:225], v156 offset:16384\n"
      "ds_read_b64_tr_b16 v[226:227], v157 offset:16384\n"
      "ds_read_b64_tr_b16 v[228:229], v156 offset:16392\n"
      "ds_read_b64_tr_b16 v[230:231], v157 offset:16392\n"
      "ds_read_b64_tr_b16 v[232:233], v156 offset:24576\n"
      "ds_read_b64_tr_b16 v[234:235], v157 offset:24576\n"
      "ds_read_b64_tr_b16 v[236:237], v156 offset:24584\n"
      "ds_read_b64_tr_b16 v[238:239], v157 offset:24584\n"
      "s_setpc_b64 s[50:51]\n"
      "s_nop 0\n"
      "ds_read_b64_tr_b16 v[240:241], v156 offset:16384\n"
      "ds_read_b64_tr_b16 v[242:243], v157 offset:16384\n"
      "ds_read_b64_tr_b16 v[244:245], v156 offset:16392\n"
      "ds_read_b64_tr_b16 v[246:247], v157 offset:16392\n"
      "ds_read_b64_tr_b16 v[248:249], v156 offset:24576\n"
      "ds_read_b64_tr_b16 v[250:251], v157 offset:24576\n"
      "ds_read_b64_tr_b16 v[252:253], v156 offset:24584\n"
      "ds_read_b64_tr_b16 v[254:255], v157 offset:24584\n"
      "s_setpc_b64 s[50:51]\n"
      "s_nop 0\n"
      "s_setpc_b64 s[50:51]\n"
      "s_nop 0\n"
      "s_nop 0\n"
      "s_nop 0\n"
      "s_nop 0\n"
      "s_nop 0\n"
      "s_nop 0\n"
      "s_nop 0\n"
      "s_nop 0\n"
      "s_nop 0\n"
      "s_nop 0\n"
      "s_nop 0\n"
      "s_nop 0\n"
      "s_nop 0\n"
      "s_nop 0\n"
      "s_nop 0\n"
      "s_nop 0\n"
      "s_nop 0\n"
      "s_setpc_b64 s[50:51]\n"
      "s_nop 0\n"
      "s_nop 0\n"
      "s_nop 0\n"
      "s_nop 0\n"
      "s_nop 0\n"
      "s_nop 0\n"
      "s_nop 0\n"
      "s_nop 0\n"
      "s_nop 0\n"
      "s_nop 0\n"
      "s_nop 0\n"
      "s_nop 0\n"
      "s_nop 0\n"
      "s_nop 0\n"
      "s_nop 0\n"
      "s_nop 0\n"
      "s_nop 0\n"
      "s_setpc_b64 s[50:51]\n"
      "s_nop 0\n"
      "s_nop 0\n"
      "s_nop 0\n"
      "s_nop 0\n"
      "s_nop 0\n"
      "s_nop 0\n"
      "s_nop 0\n"
      "s_nop 0\n"
      "s_nop 0\n"
      "s_nop 0\n"
      "s_nop 0\n"
      "s_nop 0\n"
      "s_nop 0\n"
      "s_nop 0\n"
      "s_nop 0\n"
      "s_nop 0\n"
      "s_nop 0\n"
      ".Lgq_bl_rare:\n"
      "s_getpc_b64 s[50:51]\n"
      "s_branch .Lgq_rescale1\n"
      "s_branch .Lgq_bl_ret\n"
      ".Lgq_x_hint:\n"
      "s_mov_b32 s87, 65536\n"
      ".Lgq_x_hspin:\n"
      "buffer_load_dword v13, off, s[40:43], s57 offset:2060 sc1\n"
      "s_waitcnt vmcnt(0)\n"
      "v_cmp_eq_u32 vcc, s48, v13\n"
      "s_nop 1\n"
      "s_cmp_eq_u64 vcc, exec\n"
      "s_cbranch_scc1 .Lgq_x_hok\n"
      "s_sub_u32 s87, s87, 1\n"
      "s_cmp_lg_u32 s87, 0\n"
      "s_cbranch_scc1 .Lgq_x_hspin\n"
      ".Lgq_x_hok:\n"
      "buffer_load_dwordx4 v[176:179], v170, s[40:43], s56 offen offset:0 sc1\n"
      "buffer_load_dwordx4 v[180:183], v170, s[40:43], s56 offen offset:1024 sc1\n"
      "buffer_load_dwordx4 v[184:187], v170, s[40:43], s56 offen offset:2048 sc1\n"
      "buffer_load_dwordx4 v[188:191], v170, s[40:43], s56 offen offset:3072 sc1\n"
      "buffer_load_dwordx4 v[192:195], v170, s[40:43], s80 offen offset:0 sc1\n"
      "buffer_load_dwordx4 v[196:199], v170, s[40:43], s80 offen offset:1024 sc1\n"
      "buffer_load_dwordx4 v[200:203], v170, s[40:43], s80 offen offset:2048 sc1\n"
      "buffer_load_dwordx4 v[204:207], v170, s[40:43], s80 offen offset:3072 sc1\n"
      "buffer_load_dwordx4 v[132:135], v170, s[40:43], s81 offen offset:0 sc1\n"
      "buffer_load_dwordx4 v[136:139], v170, s[40:43], s81 offen offset:1024 sc1\n"
      "buffer_load_dwordx4 v[140:143], v170, s[40:43], s81 offen offset:2048 sc1\n"
      "s_branch .Lgq_x_have\n"
      ".Lgq_x_slow:\n"
      "s_mov_b32 s87, 65536\n"
      ".Lgq_x_spin:\n"
      "buffer_load_dwordx4 v[176:179], v170, s[40:43], s56 offen offset:0 sc1\n"
      "buffer_load_dwordx4 v[180:183], v170, s[40:43], s56 offen offset:1024 sc1\n"
      "buffer_load_dwordx4 v[184:187], v170, s[40:43], s56 offen offset:2048 sc1\n"
      "buffer_load_dwordx4 v[188:191], v170, s[40:43], s56 offen offset:3072 sc1\n"
      "buffer_load_dwordx4 v[192:195], v170, s[40:43], s80 offen offset:0 sc1\n"
      "buffer_load_dwordx4 v[196:199], v170, s[40:43], s80 offen offset:1024 sc1\n"
      "buffer_load_dwordx4 v[200:203], v170, s[40:43], s80 offen offset:2048 sc1\n"
      "buffer_load_dwordx4 v[204:207], v170, s[40:43], s80 offen offset:3072 sc1\n"
      "buffer_load_dwordx4 v[132:135], v170, s[40:43], s81 offen offset:0 sc1\n"
      "buffer_load_dwordx4 v[136:139], v170, s[40:43], s81 offen offset:1024 sc1\n"
      "buffer_load_dwordx4 v[140:143], v170, s[40:43], s81 offen offset:2048 sc1\n"
      "s_waitcnt vmcnt(0)\n"
      "v_min3_u32 v2, v179, v183, v187\n"
      "v_max3_u32 v3, v179, v183, v187\n"
      "v_min3_u32 v2, v2, v191, v195\n"
      "v_max3_u32 v3, v3, v191, v195\n"
      "v_min3_u32 v2, v2, v199, v203\n"
      "v_max3_u32 v3, v3, v199, v203\n"
      "v_min3_u32 v2, v2, v207, v135\n"
      "v_max3_u32 v3, v3, v207, v135\n"
      "v_min3_u32 v2, v2, v139, v143\n"
      "v_max3_u32 v3, v3, v139, v143\n"
      "s_mov_b32 s54, s48\n"
      "s_mov_b32 s55, s48\n"
      "v_cmp_eq_u64 vcc, s[54:55], v[2:3]\n"
      "s_nop 1\n"
      "s_cmp_eq_u64 vcc, exec\n"
      "s_cbranch_scc1 .Lgq_x_go\n"
      "s_sub_u32 s87, s87, 1\n"
      "s_cmp_lg_u32 s87, 0\n"
      "s_cbranch_scc1 .Lgq_x_spin\n"
      "s_branch .Lgq_x_go\n"
      ".Lgq_end:\n"
      :
      : "s"(__builtin_amdgcn_kernarg_segment_ptr()), "s"(blockIdx.x), "v"(tid)
      : "memory", "vcc", "scc", "s4", "s5", "s6", "s7", "s8", "s9", "s10", "s11", "s12", "s13", "s14", "s15", "s16", "s17", "s18", "s19", "s20", "s21", "s22", "s23", "s24", "s25", "s26", "s27",
        "s28", "s29", "s30", "s31", "s32", "s33", "s34", "s35", "s36", "s37", "s38", "s39", "s40", "s41", "s42", "s43", "s44", "s45", "s46", "s47", "s48", "s49", "s50", "s51",
        "s52", "s53", "s54", "s55", "s56", "s57", "s58", "s59", "s60", "s61", "s62", "s63", "s64", "s65", "s66", "s67", "s68", "s69", "s70", "s71", "s72", "s73", "s74", "s75",
        "s76", "s77", "s78", "s79", "s80", "s81", "s82", "s83", "s84", "s85", "s86", "s87", "s88", "s89", "s90", "s91", "s92", "s93", "s94", "s95", "s96", "s97", "s98", "s99",
        "v1", "v2", "v3", "v4", "v5", "v6", "v7", "v8", "v9", "v10", "v11", "v12", "v13", "v14", "v15", "v16", "v17", "v18", "v19", "v20", "v21", "v22", "v23", "v24",
        "v25", "v26", "v27", "v28", "v29", "v30", "v31", "v32", "v33", "v34", "v35", "v36", "v37", "v38", "v39", "v40", "v41", "v42", "v43", "v44", "v45", "v46", "v47", "v48",
        "v49", "v50", "v51", "v52", "v53", "v54", "v55", "v56", "v57", "v58", "v59", "v60", "v61", "v62", "v63", "v64", "v65", "v66", "v67", "v68", "v69", "v70", "v71", "v72",
        "v73", "v74", "v75", "v76", "v77", "v78", "v79", "v80", "v81", "v82", "v83", "v84", "v85", "v86", "v87", "v88", "v89", "v90", "v91", "v92", "v93", "v94", "v95", "v96",
        "v97", "v98", "v99", "v100", "v101", "v102", "v103", "v104", "v105", "v106", "v107", "v108", "v109", "v110", "v111", "v112", "v113", "v114", "v115", "v116", "v117", "v118", "v119", "v120",
        "v121", "v122", "v123", "v124", "v125", "v126", "v127", "v128", "v129", "v130", "v131", "v132", "v133", "v134", "v135", "v136", "v137", "v138", "v139", "v140", "v141", "v142", "v143", "v144",
        "v145", "v146", "v147", "v148", "v149", "v150", "v151", "v152", "v153", "v154", "v155", "v156", "v157", "v158", "v159", "v160", "v161", "v162", "v163", "v164", "v165", "v166", "v167", "v168",
        "v169", "v170", "v171", "v172", "v173", "v174", "v175", "v176", "v177", "v178", "v179", "v180", "v181", "v182", "v183", "v184", "v185", "v186", "v187", "v188", "v189", "v190", "v191", "v192",
        "v193", "v194", "v195", "v196", "v197", "v198", "v199", "v200", "v201", "v202", "v203", "v204", "v205", "v206", "v207", "v208", "v209", "v210", "v211", "v212", "v213", "v214", "v215", "v216",
        "v217", "v218", "v219", "v220", "v221", "v222", "v223", "v224", "v225", "v226", "v227", "v228", "v229", "v230", "v231", "v232", "v233", "v234", "v235", "v236", "v237", "v238", "v239", "v240",
        "v241", "v242", "v243", "v244", "v245", "v246", "v247", "v248", "v249", "v250", "v251", "v252", "v253", "v254", "v255", "a0", "a1", "a2", "a3", "a4", "a5", "a6", "a7", "a8",
        "a9", "a10", "a11", "a12", "a13", "a14", "a15", "a16", "a17", "a18", "a19", "a20", "a21", "a22", "a23", "a24", "a25", "a26", "a27", "a28", "a29", "a30", "a31", "a32",
        "a33", "a34", "a35", "a36", "a37", "a38", "a39", "a40", "a41", "a42", "a43", "a44", "a45", "a46", "a47", "a48", "a49", "a50", "a51", "a52", "a53", "a54", "a55", "a56",
        "a57", "a58", "a59", "a60", "a61", "a62", "a63", "a64", "a65", "a66", "a67", "a68", "a69", "a70", "a71", "a72", "a73", "a74", "a75", "a76", "a77", "a78", "a79", "a80",
        "a81", "a82", "a83", "a84", "a85", "a86", "a87", "a88", "a89", "a90", "a91", "a92", "a93", "a94", "a95", "a96", "a97", "a98", "a99", "a100", "a101", "a102", "a103", "a104",
        "a105", "a106", "a107", "a108", "a109", "a110", "a111", "a112", "a113", "a114", "a115", "a116", "a117", "a118", "a119", "a120", "a121", "a122", "a123", "a124", "a125", "a126", "a127", "a128",
        "a129", "a130", "a131", "a132", "a133", "a134", "a135", "a136", "a137", "a138", "a139", "a140", "a141", "a142", "a143", "a144", "a145", "a146", "a147", "a148", "a149", "a150", "a151", "a152",
        "a153", "a154", "a155", "a156", "a157", "a158", "a159", "a160", "a161", "a162", "a163", "a164", "a165", "a166", "a167", "a168", "a169", "a170", "a171", "a172", "a173", "a174", "a175", "a176",
        "a177", "a178", "a179", "a180", "a181", "a182", "a183", "a184", "a185", "a186", "a187", "a188", "a189", "a190", "a191", "a192", "a193", "a194", "a195", "a196", "a197", "a198", "a199", "a200",
        "a201", "a202", "a203", "a204", "a205", "a206", "a207", "a208", "a209", "a210", "a211", "a212", "a213", "a214", "a215", "a216", "a217", "a218", "a219", "a220", "a221", "a222", "a223", "a224",
        "a225", "a226", "a227", "a228", "a229", "a230", "a231", "a232", "a233", "a234", "a235", "a236", "a237", "a238", "a239", "a240", "a241", "a242", "a243", "a244", "a245", "a246", "a247", "a248",
        "a249", "a250", "a251", "a252", "a253", "a254", "a255");
}

extern "C" void kernel_launch(void* const* d_in, const int* in_sizes, int n_in, void* d_out, int out_size, void* d_ws,
                              size_t ws_size, hipStream_t stream) {
  (void)in_sizes; (void)n_in; (void)out_size; (void)ws_size;
  (void)hipFuncSetAttribute((const void*)gqa_fused, hipFuncAttributeMaxDynamicSharedMemorySize, 163840);
  hipLaunchKernelGGL(gqa_fused, dim3(256), dim3(256), 163840, stream, d_in[0], d_in[1], d_in[2], d_in[3], d_in[4], d_out, d_ws);
}
```
